```python
import torch

def _softmax(x, axis=-1):
    return torch.softmax(x, dim=axis)
def _gelu(x):
    return torch.nn.functional.gelu(x, approximate="tanh")
def _silu(x):
    return torch.nn.functional.silu(x)
def _sigmoid(x):
    return torch.sigmoid(x)
def _layernorm(x, axis=-1, eps=1e-5):
    m = x.mean(dim=axis, keepdim=True)
    v = x.var(dim=axis, keepdim=True, unbiased=False)
    return (x - m) / torch.sqrt(v + eps)
def _scatter_add(tgt, idx, src, axis=0):
    return tgt.clone().index_add_(axis, idx, src)
def _segment_sum(src, seg, n):
    out = torch.zeros((n,) + src.shape[1:], dtype=src.dtype)
    return out.index_add_(0, seg, src)
def _pad(x, pw):
    flat = tuple(v for pair in reversed(pw) for v in pair)
    return torch.nn.functional.pad(x, flat)

kernel_name = 'concat_heads__gqa_block_cached__mha_block_cached__near_ridge'

def setup_inputs():

    _w = (lambda a: (lambda t: t.to(torch.bfloat16) if t.is_floating_point() else t)(torch.as_tensor(a)))
    def _A_setup():
        B, Sq, D = 8, 32, 512
        Hq, Hkv, Sk, Dh = 32, 8, 2048, 128
        assert Hq % Hkv == 0, (Hq, Hkv)
        return {
            "x": _w((lambda *s: torch.randn(*s, dtype=torch.bfloat16) * 0.8)(B, Sq, D)),
            "k_cache": _w((lambda *s: torch.randn(*s, dtype=torch.bfloat16) * 0.8)(B, Hkv, Sk, Dh)),
            "v_cache": _w((lambda *s: torch.randn(*s, dtype=torch.bfloat16) * 0.05)(B, Hkv, Sk, Dh)),
            "wq": _w((lambda *s: torch.randn(*s, dtype=torch.bfloat16) * 0.8)(D, Hq * Dh) * (D ** -0.5)),
            "wo": _w((lambda *s: torch.randn(*s, dtype=torch.bfloat16) * 0.05)(Hq * Dh, D) * ((Hq * Dh) ** -0.5)),
        }
    _A_in = _A_setup()

    def _B_setup():
        B, Sq, D = 8, 32, 512
        H, Sk, Dh = 32, 2048, 128
        return {
            "x": _w((lambda *s: torch.randn(*s, dtype=torch.bfloat16) * 0.8)(B, Sq, D)),
            "k_cache": _w((lambda *s: torch.randn(*s, dtype=torch.bfloat16) * 0.8)(B, H, Sk, Dh)),
            "v_cache": _w((lambda *s: torch.randn(*s, dtype=torch.bfloat16) * 0.05)(B, H, Sk, Dh)),
            "wq": _w((lambda *s: torch.randn(*s, dtype=torch.bfloat16) * 0.8)(D, H * Dh) * (D ** -0.5)),
            "wo": _w((lambda *s: torch.randn(*s, dtype=torch.bfloat16) * 0.05)(H * Dh, D) * ((H * Dh) ** -0.5)),
        }
    _B_in = _B_setup()

    return {
        "A_x": _A_in["x"],
        "A_k_cache": _A_in["k_cache"],
        "A_v_cache": _A_in["v_cache"],
        "A_wq": _A_in["wq"],
        "A_wo": _A_in["wo"],
        "B_x": _B_in["x"],
        "B_k_cache": _B_in["k_cache"],
        "B_v_cache": _B_in["v_cache"],
        "B_wq": _B_in["wq"],
        "B_wo": _B_in["wo"],
    }

def reference(A_x, A_k_cache, A_v_cache, A_wq, A_wo, B_x, B_k_cache, B_v_cache, B_wq, B_wo):
    def _A_ref(x, k_cache, v_cache, wq, wo):
        B, Sq, D = x.shape
        Hq, Dh = 32, 128
        Hkv = k_cache.shape[1]
        rep = Hq // Hkv
        q = (lambda x, *d: x.permute(*d))((x @ wq).reshape(B, Sq, Hq, Dh), 0, 2, 1, 3)
        k = (lambda x, n, axis: torch.repeat_interleave(x, n, dim=axis))(k_cache, rep, axis=1)
        v = (lambda x, n, axis: torch.repeat_interleave(x, n, dim=axis))(v_cache, rep, axis=1)
        scores = torch.einsum("bhqd,bhkd->bhqk", q, k) * (Dh ** -0.5)
        p = _softmax(scores, axis=-1)
        o = torch.einsum("bhqk,bhkd->bhqd", p, v)
        return (lambda x, *d: x.permute(*d))(o, 0, 2, 1, 3).reshape(B, Sq, Hq * Dh) @ wo

    def _B_ref(x, k_cache, v_cache, wq, wo):
        B, Sq, D = x.shape
        H, Dh = 32, 128
        q = (lambda x, *d: x.permute(*d))((x @ wq).reshape(B, Sq, H, Dh), 0, 2, 1, 3)
        scores = torch.einsum("bhqd,bhkd->bhqk", q, k_cache) * (Dh ** -0.5)
        p = _softmax(scores, axis=-1)
        o = torch.einsum("bhqk,bhkd->bhqd", p, v_cache)
        return (lambda x, *d: x.permute(*d))(o, 0, 2, 1, 3).reshape(B, Sq, H * Dh) @ wo

    a = _A_ref(A_x, A_k_cache, A_v_cache, A_wq, A_wo)
    b = _B_ref(B_x, B_k_cache, B_v_cache, B_wq, B_wo)
    return torch.concatenate([a, b], axis=-1)
```

```cpp
#include <hip/hip_runtime.h>
#include <hip/hip_bf16.h>

constexpr unsigned T_Q0 = 600, T_QB = 27;
constexpr unsigned T_W3 = 1800, T_W3B = 40;
constexpr unsigned T_W0 = 3000, T_WB = 40;
constexpr int P_T0 = 520, P_A = 945, P_B = 680, P_K1 = 61;
constexpr float THR = 8.f;

typedef float    f32x16 __attribute__((ext_vector_type(16)));
typedef float    f32x4  __attribute__((ext_vector_type(4)));
typedef float    f32x2  __attribute__((ext_vector_type(2)));
typedef __bf16   bf16x8 __attribute__((ext_vector_type(8)));
typedef __bf16   bf16x2 __attribute__((ext_vector_type(2)));
typedef unsigned u32x4  __attribute__((ext_vector_type(4)));
typedef unsigned u32x2  __attribute__((ext_vector_type(2)));
typedef unsigned long long u64;
typedef __amdgpu_buffer_rsrc_t srd_t;
#define DI __device__ __forceinline__
#define LAS __attribute__((address_space(3)))
#define GAS __attribute__((address_space(1)))
#define MFMA(a, b, c) __builtin_amdgcn_mfma_f32_32x32x16_bf16((a), (b), (c), 0, 0, 0)
#define SBAR() __builtin_amdgcn_sched_barrier(0)
#define SC1 16

constexpr unsigned SR = 0, CA = 65536, STG = 114688, QF = 147456, STATS = 155648, CNT = 156672, STATS2 = 156736, JUNK = 157760, LDS_BYTES = 163840;
constexpr unsigned C_XBAR = CNT, C_SBAR = CNT + 4, C_Q0 = CNT + 8, C_Q0R = CNT + 12, C_Q1 = CNT + 16, C_P = CNT + 20, C_OB = CNT + 24, C_HOLD = CNT + 28, C_ST = CNT + 32;
constexpr unsigned A_OF = 65536, A_WS = 98304;
constexpr unsigned WS_E = 0, WS_FL = 4096, WS_FT = 32768, WS_PTS = 65536, WS_PA = 4u << 20, WS_PB = 8u << 20, WS_END = 16u << 20;
constexpr float SCL = 0.08838834764831845f * 1.4426950408889634f;
constexpr unsigned SPIN_CAP = 1u << 16;

struct Params { const char* xa; const char* ka; const char* va; const char* wqa; const char* woa; const char* xb; const char* kb; const char* vb; const char* wqb; const char* wob; char* out; char* ws; };

DI unsigned cvtpk(float lo, float hi) { f32x2 v = {lo, hi}; return __builtin_bit_cast(unsigned, __builtin_convertvector(v, bf16x2)); }
DI float bflo(unsigned u) { return __uint_as_float(u << 16); }
DI float bfhi(unsigned u) { return __uint_as_float(u & 0xffff0000u); }
DI srd_t mksrd(const void* p, unsigned bytes) { return __builtin_amdgcn_make_buffer_rsrc((void*)p, 0, bytes, 0x00020000); }
template <int N> DI void waitvm() { asm volatile("s_waitcnt vmcnt(%0)" :: "i"(N) : "memory"); }
DI unsigned opq(unsigned x) { asm volatile("" : "+v"(x)); return x; }
DI bf16x8 cat(u32x2 a, u32x2 b) { u32x4 w = {a[0], a[1], b[0], b[1]}; return __builtin_bit_cast(bf16x8, w); }
DI bf16x8 asbf(u32x4 a) { return __builtin_bit_cast(bf16x8, a); }
DI float swap32_max(float x) { auto r = __builtin_amdgcn_permlane32_swap(__float_as_uint(x), __float_as_uint(x), false, false); return fmaxf(__uint_as_float(r[0]), __uint_as_float(r[1])); }
DI float swap32_sum(float x) { auto r = __builtin_amdgcn_permlane32_swap(__float_as_uint(x), __float_as_uint(x), false, false); return __uint_as_float(r[0]) + __uint_as_float(r[1]); }
DI u32x4 pack8(const f32x16& a, int s) { return u32x4{cvtpk(a[8 * s], a[8 * s + 1]), cvtpk(a[8 * s + 2], a[8 * s + 3]), cvtpk(a[8 * s + 4], a[8 * s + 5]), cvtpk(a[8 * s + 6], a[8 * s + 7])}; }
DI u32x4 natural(u32x4 f) {
  auto a = __builtin_amdgcn_permlane32_swap(f[0], f[2], false, false); auto b = __builtin_amdgcn_permlane32_swap(f[1], f[3], false, false);
  return u32x4{a[0], b[0], a[1], b[1]};
}
DI void lds_add(char* smem, unsigned a, unsigned v) {
  unsigned ta, tv;
  asm volatile("s_mov_b64 exec, 1\n\tv_mov_b32 %0, %2\n\tv_mov_b32 %1, %3\n\tds_add_u32 %0, %1\n\ts_mov_b64 exec, -1" : "=&v"(ta), "=&v"(tv) : "s"((unsigned)(size_t)(LAS char*)smem + a), "s"(v) : "memory");
}
DI void lds_inc(char* smem, unsigned a) { lds_add(smem, a, 1); }
DI void hold_wait(char* smem) {
  for (unsigned spins = 0; spins < (1u << 20) && __builtin_amdgcn_readfirstlane(__hip_atomic_load((LAS unsigned*)(smem + C_HOLD), __ATOMIC_RELAXED, __HIP_MEMORY_SCOPE_WORKGROUP)) >= 256; ++spins) __builtin_amdgcn_s_sleep(1);
}
template <int SLEEP> DI void lds_wait(char* smem, unsigned a, unsigned target) {
  asm volatile("" ::: "memory");
  for (unsigned spins = 0; spins < (1u << 20) && (unsigned)__builtin_amdgcn_readfirstlane(__hip_atomic_load((LAS unsigned*)(smem + a), __ATOMIC_RELAXED, __HIP_MEMORY_SCOPE_WORKGROUP)) < target; ++spins) { if (SLEEP) __builtin_amdgcn_s_sleep(SLEEP); }
  asm volatile("" ::: "memory");
}
DI void st16(u32x4 v, srd_t s, unsigned vo, unsigned so) { __builtin_amdgcn_raw_buffer_store_b128(v, s, vo, so, SC1); SBAR(); asm volatile("s_nop 1"); SBAR(); }
DI void wg_bar() { asm volatile("s_waitcnt lgkmcnt(0)\n\ts_barrier" ::: "memory"); }
DI void role_bar(char* smem, unsigned a, unsigned& gen) { gen += 4; lds_inc(smem, a); lds_wait<0>(smem, a, gen); }
DI void flag_set(char* ws, unsigned off, u64 nonce, unsigned lane) {
  asm volatile("s_waitcnt vmcnt(0)" ::: "memory");
  if (lane == 0) __hip_atomic_store((GAS u64*)(ws + off), nonce, __ATOMIC_RELAXED, __HIP_MEMORY_SCOPE_AGENT);
}
template <int SLEEP> DI void flag_wait(char* ws, unsigned off_lane, u64 nonce) {
  for (unsigned spins = 0; spins < SPIN_CAP; ++spins) {
    const u64 v = __hip_atomic_load((GAS u64*)(ws + off_lane), __ATOMIC_RELAXED, __HIP_MEMORY_SCOPE_AGENT);
    if (__all(v == nonce)) break;
    __builtin_amdgcn_s_sleep(SLEEP);
  }
  asm volatile("" ::: "memory");
}

template <bool NT> DI void dma_half(unsigned m0a, const unsigned (&vo)[4], srd_t srd, unsigned so) {
#define DMA_H(P) asm volatile("s_mov_b32 m0, %0\n\ts_nop 0\n\tbuffer_load_dwordx4 %1, %5, %6 offen" P " lds\n\tbuffer_load_dwordx4 %2, %5, %6 offen offset:1024" P " lds\n\t" \
    "buffer_load_dwordx4 %3, %5, %6 offen offset:2048" P " lds\n\tbuffer_load_dwordx4 %4, %5, %6 offen offset:3072" P " lds" \
    :: "s"(m0a), "v"(vo[0]), "v"(vo[1]), "v"(vo[2]), "v"(vo[3]), "s"(srd), "s"(so) : "memory")
  if constexpr (NT) DMA_H(" nt"); else DMA_H("");
#undef DMA_H
}
template <bool NT> DI void dma_tile2(unsigned m0a, const unsigned (&vo)[4], srd_t srd, unsigned so, unsigned so2) { dma_half<NT>(m0a, vo, srd, so); dma_half<NT>(m0a + 4096, vo, srd, so2); }
DI void dma_kv(unsigned m0a, const unsigned (&vo)[4], u64 base, unsigned so) { dma_tile2<true>(m0a, vo, mksrd((const void*)base, 524288), so, so + 4096); }
template <unsigned OFF> DI void row_reads(u32x4 (&f)[8], const unsigned (&ka)[8]) {
  asm volatile("ds_read_b128 %0, %8 offset:%16\n\tds_read_b128 %1, %9 offset:%16\n\tds_read_b128 %2, %10 offset:%16\n\tds_read_b128 %3, %11 offset:%16\n\t"
               "ds_read_b128 %4, %12 offset:%16\n\tds_read_b128 %5, %13 offset:%16\n\tds_read_b128 %6, %14 offset:%16\n\tds_read_b128 %7, %15 offset:%16\n\t"
               "s_waitcnt lgkmcnt(0)"
               : "=&v"(f[0]), "=&v"(f[1]), "=&v"(f[2]), "=&v"(f[3]), "=&v"(f[4]), "=&v"(f[5]), "=&v"(f[6]), "=&v"(f[7])
               : "v"(ka[0]), "v"(ka[1]), "v"(ka[2]), "v"(ka[3]), "v"(ka[4]), "v"(ka[5]), "v"(ka[6]), "v"(ka[7]), "i"(OFF) : "memory");
}
template <unsigned OFF> DI void tr_reads(u32x2 (&f)[4][2][2], const unsigned (&va)[4][2]) {
  asm volatile(
      "ds_read_b64_tr_b16 %0, %16 offset:%24\n\tds_read_b64_tr_b16 %1, %17 offset:%24\n\tds_read_b64_tr_b16 %2, %16 offset:%25\n\tds_read_b64_tr_b16 %3, %17 offset:%25\n\t"
      "ds_read_b64_tr_b16 %4, %18 offset:%24\n\tds_read_b64_tr_b16 %5, %19 offset:%24\n\tds_read_b64_tr_b16 %6, %18 offset:%25\n\tds_read_b64_tr_b16 %7, %19 offset:%25\n\t"
      "ds_read_b64_tr_b16 %8, %20 offset:%24\n\tds_read_b64_tr_b16 %9, %21 offset:%24\n\tds_read_b64_tr_b16 %10, %20 offset:%25\n\tds_read_b64_tr_b16 %11, %21 offset:%25\n\t"
      "ds_read_b64_tr_b16 %12, %22 offset:%24\n\tds_read_b64_tr_b16 %13, %23 offset:%24\n\tds_read_b64_tr_b16 %14, %22 offset:%25\n\tds_read_b64_tr_b16 %15, %23 offset:%25\n\t"
      "s_waitcnt lgkmcnt(0)"
      : "=&v"(f[0][0][0]), "=&v"(f[0][0][1]), "=&v"(f[0][1][0]), "=&v"(f[0][1][1]), "=&v"(f[1][0][0]), "=&v"(f[1][0][1]), "=&v"(f[1][1][0]), "=&v"(f[1][1][1]),
        "=&v"(f[2][0][0]), "=&v"(f[2][0][1]), "=&v"(f[2][1][0]), "=&v"(f[2][1][1]), "=&v"(f[3][0][0]), "=&v"(f[3][0][1]), "=&v"(f[3][1][0]), "=&v"(f[3][1][1])
      : "v"(va[0][0]), "v"(va[0][1]), "v"(va[1][0]), "v"(va[1][1]), "v"(va[2][0]), "v"(va[2][1]), "v"(va[3][0]), "v"(va[3][1]), "i"(OFF), "i"(OFF + 4096u) : "memory");
}
template <unsigned OFF> DI void tr_issue(u32x2 (&f)[4][2][2], const unsigned (&va)[4][2]) {
  asm volatile(
      "ds_read_b64_tr_b16 %0, %16 offset:%24\n\tds_read_b64_tr_b16 %1, %17 offset:%24\n\tds_read_b64_tr_b16 %2, %16 offset:%25\n\tds_read_b64_tr_b16 %3, %17 offset:%25\n\t"
      "ds_read_b64_tr_b16 %4, %18 offset:%24\n\tds_read_b64_tr_b16 %5, %19 offset:%24\n\tds_read_b64_tr_b16 %6, %18 offset:%25\n\tds_read_b64_tr_b16 %7, %19 offset:%25\n\t"
      "ds_read_b64_tr_b16 %8, %20 offset:%24\n\tds_read_b64_tr_b16 %9, %21 offset:%24\n\tds_read_b64_tr_b16 %10, %20 offset:%25\n\tds_read_b64_tr_b16 %11, %21 offset:%25\n\t"
      "ds_read_b64_tr_b16 %12, %22 offset:%24\n\tds_read_b64_tr_b16 %13, %23 offset:%24\n\tds_read_b64_tr_b16 %14, %22 offset:%25\n\tds_read_b64_tr_b16 %15, %23 offset:%25"
      : "=&v"(f[0][0][0]), "=&v"(f[0][0][1]), "=&v"(f[0][1][0]), "=&v"(f[0][1][1]), "=&v"(f[1][0][0]), "=&v"(f[1][0][1]), "=&v"(f[1][1][0]), "=&v"(f[1][1][1]),
        "=&v"(f[2][0][0]), "=&v"(f[2][0][1]), "=&v"(f[2][1][0]), "=&v"(f[2][1][1]), "=&v"(f[3][0][0]), "=&v"(f[3][0][1]), "=&v"(f[3][1][0]), "=&v"(f[3][1][1])
      : "v"(va[0][0]), "v"(va[0][1]), "v"(va[1][0]), "v"(va[1][1]), "v"(va[2][0]), "v"(va[2][1]), "v"(va[3][0]), "v"(va[3][1]), "i"(OFF), "i"(OFF + 4096u) : "memory");
}
DI void tr_wait(u32x2 (&f)[4][2][2]) {
  asm volatile("s_waitcnt lgkmcnt(0)"
      : "+v"(f[0][0][0]), "+v"(f[0][0][1]), "+v"(f[0][1][0]), "+v"(f[0][1][1]), "+v"(f[1][0][0]), "+v"(f[1][0][1]), "+v"(f[1][1][0]), "+v"(f[1][1][1]),
        "+v"(f[2][0][0]), "+v"(f[2][0][1]), "+v"(f[2][1][0]), "+v"(f[2][1][1]), "+v"(f[3][0][0]), "+v"(f[3][0][1]), "+v"(f[3][1][0]), "+v"(f[3][1][1]) :: "memory");
}
DI void row_reads_h(u32x4 (&f)[8], const unsigned (&ka)[8], unsigned& hv, unsigned ha, u64& now) {
  asm volatile("s_memrealtime %9\n\tds_read_b128 %0, %10\n\tds_read_b128 %1, %11\n\tds_read_b128 %2, %12\n\tds_read_b128 %3, %13\n\t"
               "ds_read_b128 %4, %14\n\tds_read_b128 %5, %15\n\tds_read_b128 %6, %16\n\tds_read_b128 %7, %17\n\tds_read_b32 %8, %18\n\t"
               "s_waitcnt lgkmcnt(0)"
               : "=&v"(f[0]), "=&v"(f[1]), "=&v"(f[2]), "=&v"(f[3]), "=&v"(f[4]), "=&v"(f[5]), "=&v"(f[6]), "=&v"(f[7]), "=&v"(hv), "=&s"(now)
               : "v"(ka[0]), "v"(ka[1]), "v"(ka[2]), "v"(ka[3]), "v"(ka[4]), "v"(ka[5]), "v"(ka[6]), "v"(ka[7]), "v"(ha) : "memory");
}
DI void tr_reads_h(u32x2 (&f)[4][2][2], const unsigned (&va)[4][2], unsigned& hv, unsigned ha, u64& now) {
  asm volatile("s_memrealtime %17\n\t"
      "ds_read_b64_tr_b16 %0, %18 offset:8192\n\tds_read_b64_tr_b16 %1, %19 offset:8192\n\tds_read_b64_tr_b16 %2, %18 offset:12288\n\tds_read_b64_tr_b16 %3, %19 offset:12288\n\t"
      "ds_read_b64_tr_b16 %4, %20 offset:8192\n\tds_read_b64_tr_b16 %5, %21 offset:8192\n\tds_read_b64_tr_b16 %6, %20 offset:12288\n\tds_read_b64_tr_b16 %7, %21 offset:12288\n\t"
      "ds_read_b64_tr_b16 %8, %22 offset:8192\n\tds_read_b64_tr_b16 %9, %23 offset:8192\n\tds_read_b64_tr_b16 %10, %22 offset:12288\n\tds_read_b64_tr_b16 %11, %23 offset:12288\n\t"
      "ds_read_b64_tr_b16 %12, %24 offset:8192\n\tds_read_b64_tr_b16 %13, %25 offset:8192\n\tds_read_b64_tr_b16 %14, %24 offset:12288\n\tds_read_b64_tr_b16 %15, %25 offset:12288\n\t"
      "ds_read_b32 %16, %26\n\ts_waitcnt lgkmcnt(0)"
      : "=&v"(f[0][0][0]), "=&v"(f[0][0][1]), "=&v"(f[0][1][0]), "=&v"(f[0][1][1]), "=&v"(f[1][0][0]), "=&v"(f[1][0][1]), "=&v"(f[1][1][0]), "=&v"(f[1][1][1]),
        "=&v"(f[2][0][0]), "=&v"(f[2][0][1]), "=&v"(f[2][1][0]), "=&v"(f[2][1][1]), "=&v"(f[3][0][0]), "=&v"(f[3][0][1]), "=&v"(f[3][1][0]), "=&v"(f[3][1][1]), "=&v"(hv), "=&s"(now)
      : "v"(va[0][0]), "v"(va[0][1]), "v"(va[1][0]), "v"(va[1][1]), "v"(va[2][0]), "v"(va[2][1]), "v"(va[3][0]), "v"(va[3][1]), "v"(ha) : "memory");
}
DI void stage_tile(const u32x4 (&r)[8], const unsigned (&sa)[4]) {
  asm volatile("ds_write_b128 %8, %0\n\tds_write_b128 %9, %1\n\tds_write_b128 %10, %2\n\tds_write_b128 %11, %3\n\t"
               "ds_write_b128 %8, %4 offset:4096\n\tds_write_b128 %9, %5 offset:4096\n\tds_write_b128 %10, %6 offset:4096\n\tds_write_b128 %11, %7 offset:4096"
               :: "v"(r[0]), "v"(r[1]), "v"(r[2]), "v"(r[3]), "v"(r[4]), "v"(r[5]), "v"(r[6]), "v"(r[7]), "v"(sa[0]), "v"(sa[1]), "v"(sa[2]), "v"(sa[3]) : "memory");
}
template <unsigned RS> DI void load_tile(u32x4 (&r)[8], srd_t s, unsigned vo, unsigned so) {
#pragma unroll
  for (int j = 0; j < 8; ++j) r[j] = __builtin_amdgcn_raw_buffer_load_b128(s, vo, so + j * 4 * RS, 0);
}
DI void pv_mfma(f32x16 (&acc)[4], const u32x2 (&f)[4][2][2], u32x4 pb0, u32x4 pb1) {
#pragma unroll
  for (int c = 0; c < 4; ++c) acc[c] = MFMA(cat(f[c][0][0], f[c][0][1]), asbf(pb0), acc[c]);
#pragma unroll
  for (int c = 0; c < 4; ++c) acc[c] = MFMA(cat(f[c][1][0], f[c][1][1]), asbf(pb1), acc[c]);
}
DI void mk_ka(unsigned (&ka)[8], unsigned base, unsigned l, unsigned sx) {
  const unsigned r = l & 31, h = l >> 5, fr = ((r & 3) << 2) | ((r >> 2) & 3);
#pragma unroll
  for (unsigned s = 0; s < 8; ++s) ka[s] = base + 256 * r + 16 * ((2 * (s ^ sx) + h) ^ fr);
}
DI void mk_va(unsigned (&va)[4][2], unsigned base, unsigned l, unsigned w) {
  const unsigned h = l >> 5, blk = (l >> 4) & 1, q = (l & 15) >> 2, p = l & 3;
#pragma unroll
  for (unsigned c = 0; c < 4; ++c)
#pragma unroll
    for (unsigned t = 0; t < 2; ++t) va[c][t] = base + 256 * (8 * t + 4 * h + q) + 16 * (((c ^ w ^ q) << 2) | ((2 * blk + (p >> 1)) ^ (2 * t + h))) + 8 * (p & 1);
}
DI unsigned mk_vo(unsigned l, unsigned j) { return (l >> 4) * 256 + 16 * ((l & 15) ^ ((l >> 4) << 2) ^ j); }
DI void st_tile(char* p, const f32x16& a) {
#pragma unroll
  for (int i = 0; i < 4; ++i) *(f32x4*)(p + i * 1024) = f32x4{a[4 * i], a[4 * i + 1], a[4 * i + 2], a[4 * i + 3]};
}
DI void add_tile(f32x16& a, const char* p, float f) {
#pragma unroll
  for (int i = 0; i < 4; ++i) { const f32x4 v = *(const f32x4*)(p + i * 1024); a[4 * i] += f * v[0]; a[4 * i + 1] += f * v[1]; a[4 * i + 2] += f * v[2]; a[4 * i + 3] += f * v[3]; }
}
DI void qk_softmax(const u32x4 (&kf)[8], const u32x4 (&qf)[8], f32x16 (&acc)[4], float& m, float& lsum, u32x4& p0, u32x4& p1) {
  f32x16 s = {};
#pragma unroll
  for (int i = 0; i < 8; ++i) s = MFMA(asbf(kf[i]), asbf(qf[i]), s);
  float mx = fmaxf(s[0], s[1]);
#pragma unroll
  for (int r = 2; r < 16; r += 2) mx = fmaxf(fmaxf(mx, s[r]), s[r + 1]);
  mx = swap32_max(mx);
  if (__any((mx - m) * SCL > THR)) {
    const float mn = fmaxf(m, mx), alpha = __builtin_amdgcn_exp2f((m - mn) * SCL); m = mn; lsum *= alpha;
#pragma unroll
    for (int c = 0; c < 4; ++c) acc[c] *= alpha;
  }
  const float nm = -m * SCL; float ps0 = 0.f, ps1 = 0.f;
#pragma unroll
  for (int r = 0; r < 16; r += 2) { s[r] = __builtin_amdgcn_exp2f(fmaf(s[r], SCL, nm)); s[r + 1] = __builtin_amdgcn_exp2f(fmaf(s[r + 1], SCL, nm)); ps0 += s[r]; ps1 += s[r + 1]; }
  lsum += ps0 + ps1;
  p0 = pack8(s, 0); p1 = pack8(s, 1);
}

DI u64 get_nonce(const Params& p, int b) { u64 e; asm volatile("global_load_dwordx2 %0, %1, %2 sc1" : "=v"(e) : "v"(0u), "s"(p.ws + WS_E + b * 256) : "memory"); return e; }
DI u64 fix_nonce(u64 e) {
  asm volatile("" : "+v"(e) :: "memory");
  u64 n = (((u64)(unsigned)__builtin_amdgcn_readfirstlane((unsigned)(e >> 32)) << 32) | (unsigned)__builtin_amdgcn_readfirstlane((unsigned)e)) + 0x9E3779B97F4A7C15ull;
  asm volatile("" : "+s"(n)); return n;
}
DI void wait_until(u64 t0, unsigned ticks) { while (__builtin_amdgcn_s_memrealtime() - t0 < ticks) __builtin_amdgcn_s_sleep(8); }
template <int NS> DI void merge(const u32x4 (&v)[NS][2], const f32x2 (&st)[NS], f32x16& o, float& M, float& L) {
  M = st[0][0];
#pragma unroll
  for (int s = 1; s < NS; ++s) M = fmaxf(M, st[s][0]);
  float e[NS]; L = 0.f;
#pragma unroll
  for (int s = 0; s < NS; ++s) { e[s] = st[s][1] * __builtin_amdgcn_exp2f((st[s][0] - M) * SCL); L += e[s]; }
  const float inv = 1.f / L; o = f32x16{};
#pragma unroll
  for (int s = 0; s < NS; ++s) { const float f = e[s] * inv;
#pragma unroll
    for (int j = 0; j < 8; ++j) { o[2 * j] += f * bflo(v[s][j >> 2][j & 3]); o[2 * j + 1] += f * bfhi(v[s][j >> 2][j & 3]); } }
}
template <int N> DI void sum_out(const u32x4* v, srd_t so, unsigned op, unsigned ob) {
  float s[8] = {};
  unsigned c0 = 0x00003f80u, c1 = 0x3f800000u; asm volatile("" : "+s"(c0), "+s"(c1));
#pragma unroll
  for (int i = 0; i < N; ++i)
#pragma unroll
    for (int j = 0; j < 4; ++j) { const unsigned u = v[i][j];
      s[2 * j] = __builtin_amdgcn_fdot2_f32_bf16(__builtin_bit_cast(bf16x2, u), __builtin_bit_cast(bf16x2, c0), s[2 * j], false);
      s[2 * j + 1] = __builtin_amdgcn_fdot2_f32_bf16(__builtin_bit_cast(bf16x2, u), __builtin_bit_cast(bf16x2, c1), s[2 * j + 1], false); }
#pragma unroll
  for (int j = 0; j < 4; ++j) { auto r = __builtin_amdgcn_permlane16_swap(__float_as_uint(s[j]), __float_as_uint(s[j + 4]), false, false); s[j] = __uint_as_float(r[0]) + __uint_as_float(r[1]); }
#pragma unroll
  for (int j = 0; j < 2; ++j) { auto r = __builtin_amdgcn_permlane32_swap(__float_as_uint(s[j]), __float_as_uint(s[j + 2]), false, false); s[j] = __uint_as_float(r[0]) + __uint_as_float(r[1]); }
  __builtin_amdgcn_raw_buffer_store_b32(cvtpk(s[0], s[1]), so, op, ob, 0);
}
DI void final_sum(const Params& p, srd_t sws, int b, int h, int w, unsigned l, u64 nonce) {
  const srd_t so = mksrd(p.out, 524288);
  const unsigned lg = l >> 4, vo = lg * 32768 + (l & 15) * 16 + w * 256, fo = WS_FL + (b * 4 + (h >> 3)) * 512 + (l < 40 ? l : 39) * 8;
  const unsigned vob = lg == 3 ? 0x40000000u : vo;
  const unsigned op = (l & 15) * 2048 + ((l >> 4) & 1) * 16 + (l >> 5) * 4, ob = (b * 32 + 16 * (w & 1)) * 2048 + (16 * h + 4 * (w >> 1)) * 2;
  u32x4 v[10]; unsigned got = 0;
#pragma unroll
  for (int i = 0; i < 10; ++i) v[i] = u32x4{0, 0, 0, 0};
  for (unsigned spins = 0; spins < SPIN_CAP && (got & 1023) != 1023; ++spins) {
    const u64 up = __ballot(__hip_atomic_load((GAS u64*)(p.ws + fo), __ATOMIC_RELAXED, __HIP_MEMORY_SCOPE_AGENT) == nonce);
    asm volatile("" ::: "memory");
    if ((got >> 8) == 3) { got |= 1024; sum_out<2>(v + 8, so, op, ob); }
#pragma unroll
    for (int i = 0; i < 10; ++i)
      if (((up >> (4 * i)) & (i < 8 ? 7 : 15)) == (i < 8 ? 7 : 15) && !(got & (1u << i))) { got |= 1u << i;
        v[i] = __builtin_amdgcn_raw_buffer_load_b128(sws, (i < 8 ? vob : vo) + ((h + (i < 8 ? 4 * i + lg : 16 * (i - 8) + 4 * lg)) & 31) * 1024,
                                                     i < 8 ? WS_PB + (unsigned)(b * 32 + 4 * i) * 32768 : WS_PA + (unsigned)(b * 8 + 4 * (i - 8)) * 32768, SC1); }
  }
  sum_out<8>(v, so, op, ob + 1024);
  if (!(got & 1024)) sum_out<2>(v + 8, so, op, ob);
  if (h == 0 && w == 0 && l == 0) __hip_atomic_store((GAS u64*)(p.ws + WS_E + b * 256), nonce, __ATOMIC_RELAXED, __HIP_MEMORY_SCOPE_AGENT);
}

DI void l2_touch(unsigned lds0, srd_t s, unsigned vo, unsigned so) {
  asm volatile("s_mov_b32 m0, %0\n\ts_nop 4\n\tbuffer_load_dword %1, %2, %3 offen lds" :: "s"(lds0 + JUNK), "v"(vo), "s"(s), "s"(so) : "memory");
}
DI void l2_warm(const Params& p, unsigned lds0, int part, int x, unsigned l) {
  const bool q = part < 64, o = part < 32;
  l2_touch(lds0, mksrd((const void*)(q ? (u64)p.wqb : (u64)p.wob), 4096 * 1024), q ? (l >> 3) * 8192 + (l & 7) * 128 : l * 128,
           __builtin_amdgcn_readfirstlane(q ? part * 65536 + x * 1024 : x * 524288 + (part - 64) * 8192));
  l2_touch(lds0, mksrd((const void*)(o ? (u64)p.wob : (u64)p.woa), 4096 * 1024), l * 128, __builtin_amdgcn_readfirstlane(x * 524288 + (o ? part + 32 : part - 32) * 8192));
}
DI void role_s(const Params& p, char* smem, unsigned lds0, int w, unsigned l, int x, int b, int g) {
  const u64 k3 = (u64)p.kb + (size_t)(b * 32 + 4 * x + 3) * 524288, v3 = (u64)p.vb + (size_t)(b * 32 + 4 * x + 3) * 524288;
  const u64 k1 = (u64)p.kb + (size_t)(b * 32 + 4 * x + g) * 524288, v1 = (u64)p.vb + (size_t)(b * 32 + 4 * x + g) * 524288;
  const int len = g == 2 ? 22 : 21, n1 = (len - w + 3) >> 2, n = n1 + 16;
  const unsigned o3 = (21 * g + w) * 8192, o1 = (((w - len) & 3) - 4 * n1) * 8192;
  const unsigned ring = lds0 + SR + w * 16384;
  unsigned vo[4];
#pragma unroll
  for (int j = 0; j < 4; ++j) vo[j] = mk_vo(l, j);
  const bool wfirst = (b & 1) == 0;
  const unsigned ts = (unsigned)__builtin_amdgcn_s_memrealtime();
  if (!wfirst) { dma_kv(ring, vo, k3, o3); dma_kv(ring + 8192, vo, v3, o3); asm volatile("s_barrier" ::: "memory"); }
  asm volatile("s_barrier" ::: "memory");
  if (wfirst) { dma_kv(ring, vo, k3, o3); dma_kv(ring + 8192, vo, v3, o3); l2_warm(p, lds0, (b * 3 + g) * 4 + w, x, l); }
  f32x16 acc[4] = {}; float m = -1e30f, lsum = 0.f;
  unsigned ka[8], va[4][2]; mk_ka(ka, ring, l, 0); mk_va(va, ring, l, w);
  lds_wait<1>(smem, C_Q0, 4);
  u32x4 qf[8];
#pragma unroll
  for (int s = 0; s < 8; ++s) qf[s] = *(const u32x4*)(smem + QF + s * 1024 + l * 16);
  lds_inc(smem, C_Q0R);
  bool earlyV = true;
#pragma unroll 1
  for (int i = 0; i < n; ++i) {
    if (i == n1) {
      lds_wait<1>(smem, C_Q1, 4);
      const unsigned ll = opq(l); lsum = swap32_sum(lsum); const float inv = 1.f / lsum;
#pragma unroll
      for (int c = 0; c < 4; ++c) { acc[c] *= inv;
        *(u32x4*)(smem + CA + w * 8192 + (2 * (c ^ w)) * 1024 + ll * 16) = pack8(acc[c], 0); *(u32x4*)(smem + CA + w * 8192 + (2 * (c ^ w) + 1) * 1024 + ll * 16) = pack8(acc[c], 1);
        acc[c] = f32x16{}; }
      if (ll < 32) *(f32x2*)(smem + STATS2 + w * 256 + ll * 8) = f32x2{m, lsum};
      lds_inc(smem, C_P);
#pragma unroll
      for (int s = 0; s < 8; ++s) qf[s] = *(const u32x4*)(smem + QF + s * 1024 + ll * 16);
      m = -1e30f; lsum = 0.f;
    }
    const bool t0 = i < n1, t1 = i + 1 < n1, more = i + 1 < n;
    const unsigned so = (i * 32768 + (t0 ? o3 : o1)) & 0x7ffff, sn = ((i + 1) * 32768 + (t1 ? o3 : o1)) & 0x7ffff;
    const u64 vc = t0 ? v3 : v1, kn = t1 ? k3 : k1, vn = t1 ? v3 : v1;
    u32x4 kf[8], p0, p1; unsigned hv; u64 now;
    const int k4 = 4 * i + w, due = P_T0 + (k4 < P_K1 ? k4 * P_A : P_K1 * P_A + (k4 - P_K1) * P_B) / 16;
    if (earlyV) waitvm<8>(); else { waitvm<0>(); hold_wait(smem); dma_kv(ring + 8192, vo, vc, so); }
    row_reads_h(kf, ka, hv, lds0 + C_HOLD, now);
    const bool earlyK = more && __builtin_amdgcn_readfirstlane(hv) == 0 && (int)((unsigned)now - ts) >= due;
    if (earlyK) dma_kv(ring, vo, kn, sn);
    SBAR();
    qk_softmax(kf, qf, acc, m, lsum, p0, p1);
    if (!more) { lsum = swap32_sum(lsum); *(f32x2*)(smem + STATS + w * 256 + (l & 31) * 8) = f32x2{m, lsum}; lds_inc(smem, C_ST); }
    SBAR();
    if (earlyK) waitvm<8>(); else { waitvm<0>(); if (more) { hold_wait(smem); dma_kv(ring, vo, kn, sn); } }
    u32x2 vf[4][2][2]; tr_reads_h(vf, va, hv, lds0 + C_HOLD, now);
    earlyV = more && __builtin_amdgcn_readfirstlane(hv) == 0 && (int)((unsigned)now - ts) >= due + 2 * P_A / 16;
    if (earlyV) dma_kv(ring + 8192, vo, vn, sn);
    SBAR();
    pv_mfma(acc, vf, p0, p1);
  }
  l = opq(l);
  float f[4];
  { lds_wait<0>(smem, C_ST, 4);
    f32x2 st[4]; float M = m;
#pragma unroll
    for (int k = 1; k < 4; ++k) { st[k] = *(const f32x2*)(smem + STATS + (w ^ k) * 256 + (l & 31) * 8); M = fmaxf(M, st[k][0]); }
    f[0] = __builtin_amdgcn_exp2f((m - M) * SCL); float L = f[0] * lsum;
#pragma unroll
    for (int k = 1; k < 4; ++k) { f[k] = __builtin_amdgcn_exp2f((st[k][0] - M) * SCL); L += f[k] * st[k][1]; }
    const float inv = __builtin_amdgcn_rcpf(L);
#pragma unroll
    for (int k = 0; k < 4; ++k) f[k] *= inv; }
  SBAR();
#pragma unroll
  for (int k = 1; k < 4; ++k) st_tile(smem + SR + w * 16384 + (k - 1) * 4096 + l * 16, acc[k]);
  lds_inc(smem, C_SBAR);
  f32x16 o = acc[0] * f[0];
  unsigned a1 = SR + (w ^ 1) * 16384 + l * 16, a2 = SR + (w ^ 2) * 16384 + 4096 + l * 16, a3 = SR + (w ^ 3) * 16384 + 8192 + l * 16;
  asm volatile("" : "+v"(o), "+v"(a1), "+v"(a2), "+v"(a3));
  lds_wait<0>(smem, C_SBAR, 4);
  { f32x4 t[3][4];
#pragma unroll
    for (int i = 0; i < 4; ++i) { t[0][i] = *(const f32x4*)(smem + a1 + i * 1024); t[1][i] = *(const f32x4*)(smem + a2 + i * 1024); t[2][i] = *(const f32x4*)(smem + a3 + i * 1024); }
    SBAR();
#pragma unroll
    for (int k = 0; k < 3; ++k)
#pragma unroll
      for (int i = 0; i < 16; ++i) o[i] += f[k + 1] * t[k][i >> 2][i & 3]; }
  *(u32x4*)(smem + QF + w * 2048 + l * 16) = pack8(o, 0);
  *(u32x4*)(smem + QF + w * 2048 + 1024 + l * 16) = pack8(o, 1);
  lds_inc(smem, C_OB);
}

DI void load_wo(u32x2 (&wf)[4][4][2][2], char* smem, const char* wo, int h, int w, unsigned l, const unsigned (&sa)[4], const unsigned (&va)[4][2]) {
  const srd_t s = mksrd(wo, 4096 * 1024); const unsigned vo = (l >> 4) * 1024 + (l & 15) * 16, so = h * 131072 + w * 256;
  u32x4 r[4][8];
  lds_add(smem, C_HOLD, 256); SBAR();
#pragma unroll
  for (int i = 0; i < 3; ++i) load_tile<1024>(r[i], s, vo, so + i * 32768);
  SBAR();
  stage_tile(r[0], sa); SBAR();
  load_tile<1024>(r[3], s, vo, so + 3 * 32768);
  SBAR(); lds_add(smem, C_HOLD, -256);
  tr_reads<0>(wf[0], va); SBAR();
#pragma unroll
  for (int i = 1; i < 4; ++i) { stage_tile(r[i], sa); tr_reads<0>(wf[i], va); SBAR(); }
}
template <int MODE> DI void q_proj(const Params& p, int part, int x, const char* wq, const char* xb, int h, int rot, bool bar, char* smem, unsigned lds0, int w, unsigned l, unsigned& gen) {
  const srd_t sw = mksrd(wq + h * 256, 512 * 8192 - h * 256), sx = mksrd(xb, 32768);
  u32x4 r[4][8], xf[4][2]; f32x4 acc[8][2] = {};
  { unsigned v1k[4];
#pragma unroll
    for (int j = 0; j < 4; ++j) v1k[j] = mk_vo(l, j) + (l >> 4) * 768 + j * 3072;
    if (MODE == 0) lds_add(smem, C_HOLD, 256);
    dma_tile2<false>(lds0 + STG + w * 8192, v1k, sx, w * 256, w * 256 + 16384); }
#define WLOAD(i) _Pragma("unroll") for (int j = 0; j < 8; ++j) r[i][j] = __builtin_amdgcn_raw_buffer_load_b128(sw, (l >> 4) * 65536 + (l & 15) * 16, (w * 128 + ((i) ^ rot) * 32 + j) * 8192, 0)
  WLOAD(0); WLOAD(1);
  if (MODE == 0) { WLOAD(2); WLOAD(3); SBAR(); lds_add(smem, C_HOLD, -256); waitvm<32>(); } else { SBAR(); waitvm<16>(); }
#pragma unroll
  for (int qb = 0; qb < 2; ++qb) { const unsigned row = (l & 15) + 16 * qb, fr = ((row & 3) << 2) | ((row >> 2) & 3);
#pragma unroll
    for (int i = 0; i < 4; ++i) xf[i][qb] = *(const u32x4*)(smem + STG + w * 8192 + 256 * row + 16 * ((4 * (i ^ rot) + (l >> 4)) ^ fr)); }
#pragma unroll
  for (int i = 0; i < 4; ++i) {
#pragma unroll
    for (int t = 0; t < 8; ++t) {
      u32x4 f;
#pragma unroll
      for (int k = 0; k < 4; ++k) f[k] = __builtin_amdgcn_perm(r[i][2 * k + 1][t >> 1], r[i][2 * k][t >> 1], (t & 1) ? 0x07060302u : 0x05040100u);
      if (MODE == 1 && i == 3 && t == 0) { asm volatile("" : "+v"(f)); if (!bar) l2_warm(p, lds0, part, x, l); }
      acc[t][0] = __builtin_amdgcn_mfma_f32_16x16x32_bf16(asbf(f), asbf(xf[i][0]), acc[t][0], 0, 0, 0);
      acc[t][1] = __builtin_amdgcn_mfma_f32_16x16x32_bf16(asbf(f), asbf(xf[i][1]), acc[t][1], 0, 0, 0);
    }
    SBAR();
    if (MODE == 1 && i == 0) { WLOAD(2); SBAR(); }
    if (MODE == 1 && i == 1) { WLOAD(3); SBAR(); asm volatile("s_barrier" ::: "memory"); }
  }
#undef WLOAD
#pragma unroll
  for (int k = 0; k < 4; ++k) { char* a = smem + (k == w ? STG + w * 8192 : CA + w * 12288 + ((k ^ w) - 1) * 4096) + l * 16;
#pragma unroll
    for (int j = 0; j < 4; ++j) *(f32x4*)(a + j * 1024) = acc[2 * k + (j >> 1)][j & 1]; }
  role_bar(smem, C_XBAR, gen);
  f32x4 t[4][4];
#pragma unroll
  for (int k = 0; k < 4; ++k) { const char* a = smem + (k == w ? STG + w * 8192 : CA + k * 12288 + ((k ^ w) - 1) * 4096) + l * 16;
#pragma unroll
    for (int j = 0; j < 4; ++j) t[k][j] = *(const f32x4*)(a + j * 1024); }
  SBAR();
#pragma unroll
  for (int qb = 0; qb < 2; ++qb) { const f32x4 s0 = (t[0][qb] + t[1][qb]) + (t[2][qb] + t[3][qb]), s1 = (t[0][2 + qb] + t[1][2 + qb]) + (t[2][2 + qb] + t[3][2 + qb]);
#pragma unroll
    for (int e = 0; e < 4; ++e) *(unsigned*)(smem + QF + (2 * (l >> 4) + (e >> 1)) * 1024 + ((e & 1) * 32 + 16 * qb + (l & 15)) * 16 + 4 * w) = cvtpk(s0[e], s1[e]); }
}
DI void role_x(const Params& p, char* smem, unsigned lds0, int w, unsigned l, int x, int b, int g) {
  const int h = 4 * x + g;
  const srd_t sws = mksrd(p.ws, WS_END);
  unsigned gen = 0;
  const u64 t0 = __builtin_amdgcn_s_memrealtime();
  u64 nonce;
  const int part = (b * 3 + g) * 4 + w;
  if (threadIdx.x < 256 + 16) *(LAS unsigned*)(smem + CNT + l * 4) = 0;
  const bool wfirst = (b & 1) == 0;
  const u64 e = get_nonce(p, b);
  if (!wfirst) wg_bar(); else asm volatile("s_waitcnt lgkmcnt(0)" ::: "memory");
  __builtin_amdgcn_s_setprio(1);
  q_proj<1>(p, part, x, p.wqb, p.xb + b * 32768, 4 * x + 3, b >> 1, wfirst, smem, lds0, w, l, gen);
  lds_inc(smem, C_Q0);
  nonce = fix_nonce(e);
  wait_until(t0, T_Q0 + (b * 3 + g) * T_QB);
  lds_wait<1>(smem, C_Q0R, 4);
  q_proj<0>(p, part, x, p.wqb, p.xb + b * 32768, h, 0, false, smem, lds0, w, l, gen);
  lds_inc(smem, C_Q1);
  l = opq(l);
  const unsigned grp = (b * 8 + x) * 4;
  float M, L;
  { lds_wait<4>(smem, C_P, 4);
    u32x4 v[4][2]; f32x2 st[4];
#pragma unroll
    for (int s = 0; s < 4; ++s) { st[s] = *(const f32x2*)(smem + STATS2 + s * 256 + (l & 31) * 8);
      v[s][0] = *(const u32x4*)(smem + CA + s * 8192 + w * 2048 + l * 16); v[s][1] = *(const u32x4*)(smem + CA + s * 8192 + w * 2048 + 1024 + l * 16); }
    f32x16 o; merge<4>(v, st, o, M, L);
    *(u32x4*)(smem + CA + 32768 + (2 * w) * 1024 + l * 16) = pack8(o, 0); *(u32x4*)(smem + CA + 32768 + (2 * w + 1) * 1024 + l * 16) = pack8(o, 1);
    if (w == 0) {
      if (l < 32) __builtin_amdgcn_raw_buffer_store_b64(u32x2{__float_as_uint(M), __float_as_uint(L)}, sws, l * 8, WS_PTS + (grp + g) * 256, SC1);
      flag_set(p.ws, WS_FT + grp * 16 + g * 8, nonce, l);
    }
    role_bar(smem, C_XBAR, gen);
  }
  f32x16 y[4] = {}; u32x2 wf[4][4][2][2]; u32x4 of[8];
  unsigned va[4][2], sa[4]; mk_va(va, lds0 + STG + w * 8192, l, w);
#pragma unroll
  for (int j = 0; j < 4; ++j) sa[j] = lds0 + STG + w * 8192 + j * 1024 + mk_vo(l, j);
  wait_until(t0, T_W3 + (b * 3 + g) * T_W3B);
  load_wo(wf, smem, p.wob, 4 * x + 3, w, l, sa, va);
#pragma unroll
  for (int i = 0; i < 8; ++i) of[i] = *(const u32x4*)(smem + CA + 32768 + i * 1024 + l * 16);
#pragma unroll
  for (int c = 0; c < 4; ++c)
#pragma unroll
    for (int i = 0; i < 8; ++i) y[c] = MFMA(cat(wf[i >> 1][c][i & 1][0], wf[i >> 1][c][i & 1][1]), asbf(of[i]), y[c]);
  { flag_wait<4>(p.ws, WS_FT + grp * 16 + (l < 3 ? l : 2) * 8, nonce);
    f32x2 sg[3];
#pragma unroll
    for (int s = 0; s < 3; ++s) sg[s] = __builtin_bit_cast(f32x2, __builtin_amdgcn_raw_buffer_load_b64(sws, (l & 31) * 8, WS_PTS + (grp + s) * 256, SC1));
    const float Mg = fmaxf(fmaxf(sg[0][0], sg[1][0]), sg[2][0]);
    float Lg = 0.f;
#pragma unroll
    for (int s = 0; s < 3; ++s) Lg += sg[s][1] * __builtin_amdgcn_exp2f((sg[s][0] - Mg) * SCL);
    const float wt = L * __builtin_amdgcn_exp2f((M - Mg) * SCL) / Lg;
#pragma unroll
    for (int c = 0; c < 4; ++c) y[c] *= wt;
  }
  wait_until(t0, T_W0 + (b * 3 + g) * T_WB);
  load_wo(wf, smem, p.wob, h, w, l, sa, va);
  l = opq(l);
  lds_wait<1>(smem, C_OB, 4);
#pragma unroll
  for (int i = 0; i < 8; ++i) of[i] = *(const u32x4*)(smem + QF + i * 1024 + l * 16);
  const unsigned so = WS_PB + (unsigned)(b * 32 + h) * 32768;
#pragma unroll
  for (int c = 0; c < 5; ++c) {
    u32x4 pk;
#pragma unroll
    for (int i = 0; i < 8; ++i) {
      if (c < 4) y[c] = MFMA(cat(wf[i >> 1][c][i & 1][0], wf[i >> 1][c][i & 1][1]), asbf(of[i]), y[c]);
      if (c > 0) { pk[i & 3] = cvtpk(y[c - 1][2 * i], y[c - 1][2 * i + 1]);
        if ((i & 3) == 3) st16(pk, sws, l * 16, so + ((8 * w + 2 * ((c - 1) ^ w) + (i >> 2) + h) & 31) * 1024); }
      SBAR();
    }
  }
  flag_set(p.ws, WS_FL + (b * 4 + w) * 512 + h * 8, nonce, l);
  final_sum(p, sws, b, h, w, opq(l), nonce);
}

DI void role_a(const Params& p, char* smem, unsigned lds0, int v, unsigned l, int x, int b) {
  const int w = v & 3, u = v >> 2;
  const srd_t sws = mksrd(p.ws, WS_END);
  const unsigned wsl = A_WS + v * 8192;
  unsigned va[4][2], vo[4], v1k[4];
#pragma unroll
  for (int j = 0; j < 4; ++j) { vo[j] = mk_vo(l, j); v1k[j] = vo[j] + (l >> 4) * 768 + j * 3072; }
  const u64 kvb = (u64)((v & 2) ? p.va : p.ka) + (size_t)(b * 8 + x) * 524288;
  const srd_t skv = mksrd((const void*)kvb, 524288); const unsigned kvm = lds0 + v * 4096, kvo = u * 8192 + (v & 1) * 4096;
  f32x16 acc[4] = {};
  u32x4 qf[8];
  u64 nonce;
  { const srd_t sx = mksrd(p.xa + b * 32768, 32768), sw = mksrd(p.wqa + (4 * x + w) * 256, 512 * 8192 - (4 * x + w) * 256);
    unsigned v8k[4];
#pragma unroll
    for (int j = 0; j < 4; ++j) v8k[j] = vo[j] + (l >> 4) * 7936 + (((j & 1) << 1) | (j >> 1)) * 32768 - j * 1024;
    const u64 e = get_nonce(p, b);
    dma_half<false>(lds0 + 65536 + v * 4096, v1k, sx, (v >> 1) * 256 + (v & 1) * 16384);
    const int rot = b;
#define WQ_TILE(slot, i) dma_tile2<false>(lds0 + (slot), v8k, sw, (u * 256 + ((i) ^ rot) * 32) * 8192, (u * 256 + ((i) ^ rot) * 32 + 16) * 8192)
    WQ_TILE(v * 8192, 0); WQ_TILE(wsl, 1);
    waitvm<16>(); wg_bar();
    u32x4 xf[2][8];
    { unsigned xa[8]; mk_ka(xa, lds0 + 65536 + u * 16384 + (rot >> 2) * 8192, l, 2 * (rot & 3)); row_reads<0>(xf[0], xa);
      mk_ka(xa, lds0 + 65536 + u * 16384 + ((rot >> 2) ^ 1) * 8192, l, 2 * (rot & 3)); row_reads<0>(xf[1], xa); }
    nonce = fix_nonce(e);
    unsigned vb[4][2]; mk_va(va, lds0 + wsl, l, 2 * u); mk_va(vb, lds0 + v * 8192, l, 2 * u);
#pragma unroll
    for (int i = 0; i < 8; ++i) {
      u32x2 vf[4][2][2];
      waitvm<8>();
      if (i & 1) tr_reads<0>(vf, va); else tr_reads<0>(vf, vb);
      if (i < 6) { if (i & 1) WQ_TILE(wsl, i + 2); else WQ_TILE(v * 8192, i + 2); }
      else if (i == 6) { wg_bar(); dma_half<true>(kvm, vo, skv, kvo); dma_half<true>(kvm + 32768, vo, skv, kvo + 16384); }
      SBAR(); pv_mfma(acc, vf, xf[i >> 2][2 * (i & 3)], xf[i >> 2][2 * (i & 3) + 1]); SBAR();
    }
#undef WQ_TILE
    char* a0 = smem + wsl + l * 16; char* a1 = smem + A_WS + (v ^ 4) * 8192 + l * 16;
    st_tile(a0, acc[2]); st_tile(a0 + 4096, acc[3]);
    wg_bar();
    add_tile(acc[0], a1, 1.f); add_tile(acc[1], a1 + 4096, 1.f);
#pragma unroll
    for (int s = 0; s < 4; ++s) { qf[s] = natural(pack8(acc[s >> 1], s & 1)); *(u32x4*)(a1 + s * 1024) = qf[s]; }
    waitvm<4>(); wg_bar();
#pragma unroll
    for (int s = 0; s < 4; ++s) qf[4 + s] = *(const u32x4*)(a0 + s * 1024);
    asm volatile("" : "+v"(qf[4]), "+v"(qf[5]), "+v"(qf[6]), "+v"(qf[7]));
  }
  const srd_t swo = mksrd(p.woa + w * 256, 4096 * 1024 - w * 256);
  float m = -1e30f, lsum = 0.f;
  { unsigned ka[8]; mk_ka(ka, lds0 + u * 16384, l, 4 * u); mk_va(va, lds0 + u * 16384, l, 2 * u);
#pragma unroll
    for (int c = 0; c < 4; ++c) acc[c] = f32x16{};
    int slot = 0;
#pragma unroll 1
    for (int s = 0; s < 32; ++s) {
      if (s < 30) dma_half<true>(kvm + (slot == 0 ? 2 : slot - 1) * 32768, vo, skv, kvo + (s + 2) * 16384);
      else if (s == 30 && u == 1) { dma_tile2<false>(lds0 + wsl - 32768, v1k, swo, x * 524288, x * 524288 + 16384); dma_tile2<false>(lds0 + wsl, v1k, swo, x * 524288 + 32768, x * 524288 + 49152); }
      u32x4 kf[8], p0, p1;
      row_reads<0>(kf, ka);
      u32x2 vf[4][2][2]; tr_issue<8192>(vf, va);
      SBAR();
      qk_softmax(kf, qf, acc, m, lsum, p0, p1);
      SBAR();
      tr_wait(vf);
      SBAR();
      pv_mfma(acc, vf, p0, p1);
      const unsigned d = slot == 2 ? -65536 : 32768; slot = slot == 2 ? 0 : slot + 1;
#pragma unroll
      for (int i = 0; i < 8; ++i) { ka[i] += d; va[i >> 1][i & 1] += d; }
      if (s < 30) waitvm<4>(); else if (u == 1) waitvm<16>(); else waitvm<0>();
      wg_bar();
    }
  }
  { l = opq(l);
    lsum = swap32_sum(lsum);
    char* of = smem + A_OF + (w * 8 + 4 * u) * 1024;
    if (l < 32) *(f32x2*)(smem + A_OF + (w * 8 + 4 * (u ^ 1)) * 1024 + l * 8) = f32x2{m, lsum};
    st_tile(smem + v * 8192 + l * 16, acc[2]); st_tile(smem + v * 8192 + 4096 + l * 16, acc[3]);
    wg_bar();
    const f32x2 st = *(const f32x2*)(of + (l & 31) * 8);
    const float M = fmaxf(m, st[0]), f0 = __builtin_amdgcn_exp2f((m - M) * SCL), f1 = __builtin_amdgcn_exp2f((st[0] - M) * SCL), inv = 1.f / (f0 * lsum + f1 * st[1]);
    acc[0] *= f0 * inv; acc[1] *= f0 * inv;
    add_tile(acc[0], smem + (v ^ 4) * 8192 + l * 16, f1 * inv); add_tile(acc[1], smem + (v ^ 4) * 8192 + 4096 + l * 16, f1 * inv);
#pragma unroll
    for (int s = 0; s < 4; ++s) *(u32x4*)(of + s * 1024 + l * 16) = pack8(acc[s >> 1], s & 1);
    waitvm<8>(); wg_bar();
  }
  if (u == 1) {
#pragma unroll 1
    for (int i = 0; i < 16; ++i) {
      if (i < 14) { const unsigned so = x * 524288 + (i + 2) * 32768;
        dma_tile2<false>(lds0 + w * 8192 + (i & 1) * 32768 + ((i & 2) ? A_WS : 0), v1k, swo, so, so + 16384); waitvm<8>(); }
      else waitvm<0>();
      wg_bar();
    }
  } else {
    unsigned vb[4][2]; mk_va(va, lds0 + wsl, l, 0); mk_va(vb, lds0 + w * 8192, l, 0);
#pragma unroll
    for (int c = 0; c < 4; ++c) acc[c] = f32x16{};
#pragma unroll 1
    for (int i = 0; i < 16; i += 4) {
      const char* of = smem + A_OF + i * 2048 + l * 16;
#pragma unroll
      for (int k = 0; k < 4; ++k) {
        u32x2 vf[4][2][2];
        const u32x4 b0 = *(const u32x4*)(of + k * 2048), b1 = *(const u32x4*)(of + k * 2048 + 1024);
        if (k == 0) tr_reads<0>(vf, va); else if (k == 1) tr_reads<32768>(vf, va); else if (k == 2) tr_reads<0>(vf, vb); else tr_reads<32768>(vf, vb);
        asm volatile("s_barrier" ::: "memory");
        SBAR(); pv_mfma(acc, vf, b0, b1); SBAR();
      }
    }
  }
  if (u == 0) {
    const unsigned so = WS_PA + (unsigned)(b * 8 + x) * 32768;
#pragma unroll
    for (int c = 0; c < 4; ++c) {
      st16(pack8(acc[c], 0), sws, l * 16, so + ((8 * w + 2 * c + 4 * x) & 31) * 1024);
      st16(pack8(acc[c], 1), sws, l * 16, so + ((8 * w + 2 * c + 1 + 4 * x) & 31) * 1024);
    }
    flag_set(p.ws, WS_FL + (b * 4 + w) * 512 + 256 + x * 8, nonce, l);
  } else {
    final_sum(p, sws, b, 4 * x + 3, w, l, nonce);
  }
}

__global__ __launch_bounds__(512, 2) void fused_attn_kernel(Params p) {
  extern __shared__ __attribute__((aligned(16))) char smem[];
  const unsigned lds0 = (unsigned)(size_t)(LAS char*)smem, l = threadIdx.x & 63;
  const int wv = __builtin_amdgcn_readfirstlane(threadIdx.x >> 6), wg = blockIdx.x, x = wg & 7, g = (wg >> 3) & 3, b = wg >> 5;
  if (g == 3) role_a(p, smem, lds0, wv, l, x, b);
  else if (wv < 4) role_s(p, smem, lds0, wv, l, x, b, g);
  else role_x(p, smem, lds0, wv - 4, l, x, b, g);
}

extern "C" void kernel_launch(void* const* d_in, const int* in_sizes, int n_in, void* d_out, int out_size, void* d_ws, size_t ws_size, hipStream_t stream) {
  Params p;
  p.xa = (const char*)d_in[0]; p.ka = (const char*)d_in[1]; p.va = (const char*)d_in[2]; p.wqa = (const char*)d_in[3]; p.woa = (const char*)d_in[4];
  p.xb = (const char*)d_in[5]; p.kb = (const char*)d_in[6]; p.vb = (const char*)d_in[7]; p.wqb = (const char*)d_in[8]; p.wob = (const char*)d_in[9];
  p.out = (char*)d_out; p.ws = (char*)d_ws;
  (void)hipFuncSetAttribute((const void*)fused_attn_kernel, hipFuncAttributeMaxDynamicSharedMemorySize, LDS_BYTES);
  fused_attn_kernel<<<256, 512, LDS_BYTES, stream>>>(p);
}
```

```cpp
#include <hip/hip_runtime.h>
#include <hip/hip_bf16.h>

constexpr unsigned T_Q0 = 600, T_QB = 27;
constexpr unsigned T_W3 = 1800, T_W3B = 40;
constexpr unsigned T_W0 = 3000, T_WB = 40;
constexpr int P_T0 = 520, P_A = 945, P_B = 680, P_K1 = 61;
constexpr float THR = 8.f;

typedef float    f32x16 __attribute__((ext_vector_type(16)));
typedef float    f32x4  __attribute__((ext_vector_type(4)));
typedef float    f32x2  __attribute__((ext_vector_type(2)));
typedef __bf16   bf16x8 __attribute__((ext_vector_type(8)));
typedef __bf16   bf16x2 __attribute__((ext_vector_type(2)));
typedef unsigned u32x4  __attribute__((ext_vector_type(4)));
typedef unsigned u32x2  __attribute__((ext_vector_type(2)));
typedef unsigned long long u64;
typedef __amdgpu_buffer_rsrc_t srd_t;
#define DI __device__ __forceinline__
#define LAS __attribute__((address_space(3)))
#define GAS __attribute__((address_space(1)))
#define MFMA(a, b, c) __builtin_amdgcn_mfma_f32_32x32x16_bf16((a), (b), (c), 0, 0, 0)
#define SBAR() __builtin_amdgcn_sched_barrier(0)
#define SC1 16

constexpr unsigned SR = 0, CA = 65536, STG = 114688, QF = 147456, STATS = 155648, CNT = 156672, STATS2 = 156736, JUNK = 157760, LDS_BYTES = 163840;
constexpr unsigned C_XBAR = CNT, C_SBAR = CNT + 4, C_Q0 = CNT + 8, C_Q0R = CNT + 12, C_Q1 = CNT + 16, C_P = CNT + 20, C_OB = CNT + 24, C_HOLD = CNT + 28, C_ST = CNT + 32;
constexpr unsigned A_OF = 65536, A_WS = 98304;
constexpr unsigned WS_E = 0, WS_FL = 4096, WS_FT = 32768, WS_PTS = 65536, WS_PA = 4u << 20, WS_PB = 8u << 20, WS_END = 16u << 20;
constexpr float SCL = 0.08838834764831845f * 1.4426950408889634f;
constexpr unsigned SPIN_CAP = 1u << 16;

struct Params { const char* xa; const char* ka; const char* va; const char* wqa; const char* woa; const char* xb; const char* kb; const char* vb; const char* wqb; const char* wob; char* out; char* ws; };

DI unsigned cvtpk(float lo, float hi) { f32x2 v = {lo, hi}; return __builtin_bit_cast(unsigned, __builtin_convertvector(v, bf16x2)); }
DI float bflo(unsigned u) { return __uint_as_float(u << 16); }
DI float bfhi(unsigned u) { return __uint_as_float(u & 0xffff0000u); }
DI srd_t mksrd(const void* p, unsigned bytes) { return __builtin_amdgcn_make_buffer_rsrc((void*)p, 0, bytes, 0x00020000); }
template <int N> DI void waitvm() { asm volatile("s_waitcnt vmcnt(%0)" :: "i"(N) : "memory"); }
DI unsigned opq(unsigned x) { asm volatile("" : "+v"(x)); return x; }
DI bf16x8 cat(u32x2 a, u32x2 b) { u32x4 w = {a[0], a[1], b[0], b[1]}; return __builtin_bit_cast(bf16x8, w); }
DI bf16x8 asbf(u32x4 a) { return __builtin_bit_cast(bf16x8, a); }
DI float swap32_max(float x) { auto r = __builtin_amdgcn_permlane32_swap(__float_as_uint(x), __float_as_uint(x), false, false); return fmaxf(__uint_as_float(r[0]), __uint_as_float(r[1])); }
DI float swap32_sum(float x) { auto r = __builtin_amdgcn_permlane32_swap(__float_as_uint(x), __float_as_uint(x), false, false); return __uint_as_float(r[0]) + __uint_as_float(r[1]); }
DI u32x4 pack8(const f32x16& a, int s) { return u32x4{cvtpk(a[8 * s], a[8 * s + 1]), cvtpk(a[8 * s + 2], a[8 * s + 3]), cvtpk(a[8 * s + 4], a[8 * s + 5]), cvtpk(a[8 * s + 6], a[8 * s + 7])}; }
DI u32x4 natural(u32x4 f) {
  auto a = __builtin_amdgcn_permlane32_swap(f[0], f[2], false, false); auto b = __builtin_amdgcn_permlane32_swap(f[1], f[3], false, false);
  return u32x4{a[0], b[0], a[1], b[1]};
}
DI void lds_add(char* smem, unsigned a, unsigned v) {
  unsigned ta, tv;
  asm volatile("s_mov_b64 exec, 1\n\tv_mov_b32 %0, %2\n\tv_mov_b32 %1, %3\n\tds_add_u32 %0, %1\n\ts_mov_b64 exec, -1" : "=&v"(ta), "=&v"(tv) : "s"((unsigned)(size_t)(LAS char*)smem + a), "s"(v) : "memory");
}
DI void lds_inc(char* smem, unsigned a) { lds_add(smem, a, 1); }
DI void hold_wait(char* smem) {
  for (unsigned spins = 0; spins < (1u << 20) && __builtin_amdgcn_readfirstlane(__hip_atomic_load((LAS unsigned*)(smem + C_HOLD), __ATOMIC_RELAXED, __HIP_MEMORY_SCOPE_WORKGROUP)) >= 256; ++spins) __builtin_amdgcn_s_sleep(1);
}
template <int SLEEP> DI void lds_wait(char* smem, unsigned a, unsigned target) {
  asm volatile("" ::: "memory");
  for (unsigned spins = 0; spins < (1u << 20) && (unsigned)__builtin_amdgcn_readfirstlane(__hip_atomic_load((LAS unsigned*)(smem + a), __ATOMIC_RELAXED, __HIP_MEMORY_SCOPE_WORKGROUP)) < target; ++spins) { if (SLEEP) __builtin_amdgcn_s_sleep(SLEEP); }
  asm volatile("" ::: "memory");
}
DI void st16(u32x4 v, srd_t s, unsigned vo, unsigned so) { __builtin_amdgcn_raw_buffer_store_b128(v, s, vo, so, SC1); SBAR(); asm volatile("s_nop 1"); SBAR(); }
DI void wg_bar() { asm volatile("s_waitcnt lgkmcnt(0)\n\ts_barrier" ::: "memory"); }
DI void role_bar(char* smem, unsigned a, unsigned& gen) { gen += 4; lds_inc(smem, a); lds_wait<0>(smem, a, gen); }
DI void flag_set(char* ws, unsigned off, u64 nonce, unsigned lane) {
  asm volatile("s_waitcnt vmcnt(0)" ::: "memory");
  if (lane == 0) __hip_atomic_store((GAS u64*)(ws + off), nonce, __ATOMIC_RELAXED, __HIP_MEMORY_SCOPE_AGENT);
}
template <int SLEEP> DI void flag_wait(char* ws, unsigned off_lane, u64 nonce) {
  for (unsigned spins = 0; spins < SPIN_CAP; ++spins) {
    const u64 v = __hip_atomic_load((GAS u64*)(ws + off_lane), __ATOMIC_RELAXED, __HIP_MEMORY_SCOPE_AGENT);
    if (__all(v == nonce)) break;
    __builtin_amdgcn_s_sleep(SLEEP);
  }
  asm volatile("" ::: "memory");
}

template <bool NT> DI void dma_half(unsigned m0a, const unsigned (&vo)[4], srd_t srd, unsigned so) {
#define DMA_H(P) asm volatile("s_mov_b32 m0, %0\n\ts_nop 0\n\tbuffer_load_dwordx4 %1, %5, %6 offen" P " lds\n\tbuffer_load_dwordx4 %2, %5, %6 offen offset:1024" P " lds\n\t" \
    "buffer_load_dwordx4 %3, %5, %6 offen offset:2048" P " lds\n\tbuffer_load_dwordx4 %4, %5, %6 offen offset:3072" P " lds" \
    :: "s"(m0a), "v"(vo[0]), "v"(vo[1]), "v"(vo[2]), "v"(vo[3]), "s"(srd), "s"(so) : "memory")
  if constexpr (NT) DMA_H(" nt"); else DMA_H("");
#undef DMA_H
}
template <bool NT> DI void dma_tile2(unsigned m0a, const unsigned (&vo)[4], srd_t srd, unsigned so, unsigned so2) { dma_half<NT>(m0a, vo, srd, so); dma_half<NT>(m0a + 4096, vo, srd, so2); }
DI void dma_kv(unsigned m0a, const unsigned (&vo)[4], u64 base, unsigned so) { dma_tile2<true>(m0a, vo, mksrd((const void*)base, 524288), so, so + 4096); }
template <unsigned OFF> DI void row_reads(u32x4 (&f)[8], const unsigned (&ka)[8]) {
  asm volatile("ds_read_b128 %0, %8 offset:%16\n\tds_read_b128 %1, %9 offset:%16\n\tds_read_b128 %2, %10 offset:%16\n\tds_read_b128 %3, %11 offset:%16\n\t"
               "ds_read_b128 %4, %12 offset:%16\n\tds_read_b128 %5, %13 offset:%16\n\tds_read_b128 %6, %14 offset:%16\n\tds_read_b128 %7, %15 offset:%16\n\t"
               "s_waitcnt lgkmcnt(0)"
               : "=&v"(f[0]), "=&v"(f[1]), "=&v"(f[2]), "=&v"(f[3]), "=&v"(f[4]), "=&v"(f[5]), "=&v"(f[6]), "=&v"(f[7])
               : "v"(ka[0]), "v"(ka[1]), "v"(ka[2]), "v"(ka[3]), "v"(ka[4]), "v"(ka[5]), "v"(ka[6]), "v"(ka[7]), "i"(OFF) : "memory");
}
template <unsigned OFF> DI void tr_reads(u32x2 (&f)[4][2][2], const unsigned (&va)[4][2]) {
  asm volatile(
      "ds_read_b64_tr_b16 %0, %16 offset:%24\n\tds_read_b64_tr_b16 %1, %17 offset:%24\n\tds_read_b64_tr_b16 %2, %16 offset:%25\n\tds_read_b64_tr_b16 %3, %17 offset:%25\n\t"
      "ds_read_b64_tr_b16 %4, %18 offset:%24\n\tds_read_b64_tr_b16 %5, %19 offset:%24\n\tds_read_b64_tr_b16 %6, %18 offset:%25\n\tds_read_b64_tr_b16 %7, %19 offset:%25\n\t"
      "ds_read_b64_tr_b16 %8, %20 offset:%24\n\tds_read_b64_tr_b16 %9, %21 offset:%24\n\tds_read_b64_tr_b16 %10, %20 offset:%25\n\tds_read_b64_tr_b16 %11, %21 offset:%25\n\t"
      "ds_read_b64_tr_b16 %12, %22 offset:%24\n\tds_read_b64_tr_b16 %13, %23 offset:%24\n\tds_read_b64_tr_b16 %14, %22 offset:%25\n\tds_read_b64_tr_b16 %15, %23 offset:%25\n\t"
      "s_waitcnt lgkmcnt(0)"
      : "=&v"(f[0][0][0]), "=&v"(f[0][0][1]), "=&v"(f[0][1][0]), "=&v"(f[0][1][1]), "=&v"(f[1][0][0]), "=&v"(f[1][0][1]), "=&v"(f[1][1][0]), "=&v"(f[1][1][1]),
        "=&v"(f[2][0][0]), "=&v"(f[2][0][1]), "=&v"(f[2][1][0]), "=&v"(f[2][1][1]), "=&v"(f[3][0][0]), "=&v"(f[3][0][1]), "=&v"(f[3][1][0]), "=&v"(f[3][1][1])
      : "v"(va[0][0]), "v"(va[0][1]), "v"(va[1][0]), "v"(va[1][1]), "v"(va[2][0]), "v"(va[2][1]), "v"(va[3][0]), "v"(va[3][1]), "i"(OFF), "i"(OFF + 4096u) : "memory");
}
template <unsigned OFF> DI void tr_issue(u32x2 (&f)[4][2][2], const unsigned (&va)[4][2]) {
  asm volatile(
      "ds_read_b64_tr_b16 %0, %16 offset:%24\n\tds_read_b64_tr_b16 %1, %17 offset:%24\n\tds_read_b64_tr_b16 %2, %16 offset:%25\n\tds_read_b64_tr_b16 %3, %17 offset:%25\n\t"
      "ds_read_b64_tr_b16 %4, %18 offset:%24\n\tds_read_b64_tr_b16 %5, %19 offset:%24\n\tds_read_b64_tr_b16 %6, %18 offset:%25\n\tds_read_b64_tr_b16 %7, %19 offset:%25\n\t"
      "ds_read_b64_tr_b16 %8, %20 offset:%24\n\tds_read_b64_tr_b16 %9, %21 offset:%24\n\tds_read_b64_tr_b16 %10, %20 offset:%25\n\tds_read_b64_tr_b16 %11, %21 offset:%25\n\t"
      "ds_read_b64_tr_b16 %12, %22 offset:%24\n\tds_read_b64_tr_b16 %13, %23 offset:%24\n\tds_read_b64_tr_b16 %14, %22 offset:%25\n\tds_read_b64_tr_b16 %15, %23 offset:%25"
      : "=&v"(f[0][0][0]), "=&v"(f[0][0][1]), "=&v"(f[0][1][0]), "=&v"(f[0][1][1]), "=&v"(f[1][0][0]), "=&v"(f[1][0][1]), "=&v"(f[1][1][0]), "=&v"(f[1][1][1]),
        "=&v"(f[2][0][0]), "=&v"(f[2][0][1]), "=&v"(f[2][1][0]), "=&v"(f[2][1][1]), "=&v"(f[3][0][0]), "=&v"(f[3][0][1]), "=&v"(f[3][1][0]), "=&v"(f[3][1][1])
      : "v"(va[0][0]), "v"(va[0][1]), "v"(va[1][0]), "v"(va[1][1]), "v"(va[2][0]), "v"(va[2][1]), "v"(va[3][0]), "v"(va[3][1]), "i"(OFF), "i"(OFF + 4096u) : "memory");
}
DI void tr_wait(u32x2 (&f)[4][2][2]) {
  asm volatile("s_waitcnt lgkmcnt(0)"
      : "+v"(f[0][0][0]), "+v"(f[0][0][1]), "+v"(f[0][1][0]), "+v"(f[0][1][1]), "+v"(f[1][0][0]), "+v"(f[1][0][1]), "+v"(f[1][1][0]), "+v"(f[1][1][1]),
        "+v"(f[2][0][0]), "+v"(f[2][0][1]), "+v"(f[2][1][0]), "+v"(f[2][1][1]), "+v"(f[3][0][0]), "+v"(f[3][0][1]), "+v"(f[3][1][0]), "+v"(f[3][1][1]) :: "memory");
}
DI void row_reads_h(u32x4 (&f)[8], const unsigned (&ka)[8], unsigned& hv, unsigned ha, u64& now) {
  asm volatile("s_memrealtime %9\n\tds_read_b128 %0, %10\n\tds_read_b128 %1, %11\n\tds_read_b128 %2, %12\n\tds_read_b128 %3, %13\n\t"
               "ds_read_b128 %4, %14\n\tds_read_b128 %5, %15\n\tds_read_b128 %6, %16\n\tds_read_b128 %7, %17\n\tds_read_b32 %8, %18\n\t"
               "s_waitcnt lgkmcnt(0)"
               : "=&v"(f[0]), "=&v"(f[1]), "=&v"(f[2]), "=&v"(f[3]), "=&v"(f[4]), "=&v"(f[5]), "=&v"(f[6]), "=&v"(f[7]), "=&v"(hv), "=&s"(now)
               : "v"(ka[0]), "v"(ka[1]), "v"(ka[2]), "v"(ka[3]), "v"(ka[4]), "v"(ka[5]), "v"(ka[6]), "v"(ka[7]), "v"(ha) : "memory");
}
DI void tr_reads_h(u32x2 (&f)[4][2][2], const unsigned (&va)[4][2], unsigned& hv, unsigned ha, u64& now) {
  asm volatile("s_memrealtime %17\n\t"
      "ds_read_b64_tr_b16 %0, %18 offset:8192\n\tds_read_b64_tr_b16 %1, %19 offset:8192\n\tds_read_b64_tr_b16 %2, %18 offset:12288\n\tds_read_b64_tr_b16 %3, %19 offset:12288\n\t"
      "ds_read_b64_tr_b16 %4, %20 offset:8192\n\tds_read_b64_tr_b16 %5, %21 offset:8192\n\tds_read_b64_tr_b16 %6, %20 offset:12288\n\tds_read_b64_tr_b16 %7, %21 offset:12288\n\t"
      "ds_read_b64_tr_b16 %8, %22 offset:8192\n\tds_read_b64_tr_b16 %9, %23 offset:8192\n\tds_read_b64_tr_b16 %10, %22 offset:12288\n\tds_read_b64_tr_b16 %11, %23 offset:12288\n\t"
      "ds_read_b64_tr_b16 %12, %24 offset:8192\n\tds_read_b64_tr_b16 %13, %25 offset:8192\n\tds_read_b64_tr_b16 %14, %24 offset:12288\n\tds_read_b64_tr_b16 %15, %25 offset:12288\n\t"
      "ds_read_b32 %16, %26\n\ts_waitcnt lgkmcnt(0)"
      : "=&v"(f[0][0][0]), "=&v"(f[0][0][1]), "=&v"(f[0][1][0]), "=&v"(f[0][1][1]), "=&v"(f[1][0][0]), "=&v"(f[1][0][1]), "=&v"(f[1][1][0]), "=&v"(f[1][1][1]),
        "=&v"(f[2][0][0]), "=&v"(f[2][0][1]), "=&v"(f[2][1][0]), "=&v"(f[2][1][1]), "=&v"(f[3][0][0]), "=&v"(f[3][0][1]), "=&v"(f[3][1][0]), "=&v"(f[3][1][1]), "=&v"(hv), "=&s"(now)
      : "v"(va[0][0]), "v"(va[0][1]), "v"(va[1][0]), "v"(va[1][1]), "v"(va[2][0]), "v"(va[2][1]), "v"(va[3][0]), "v"(va[3][1]), "v"(ha) : "memory");
}
DI void stage_tile(const u32x4 (&r)[8], const unsigned (&sa)[4]) {
  asm volatile("ds_write_b128 %8, %0\n\tds_write_b128 %9, %1\n\tds_write_b128 %10, %2\n\tds_write_b128 %11, %3\n\t"
               "ds_write_b128 %8, %4 offset:4096\n\tds_write_b128 %9, %5 offset:4096\n\tds_write_b128 %10, %6 offset:4096\n\tds_write_b128 %11, %7 offset:4096"
               :: "v"(r[0]), "v"(r[1]), "v"(r[2]), "v"(r[3]), "v"(r[4]), "v"(r[5]), "v"(r[6]), "v"(r[7]), "v"(sa[0]), "v"(sa[1]), "v"(sa[2]), "v"(sa[3]) : "memory");
}
template <unsigned RS> DI void load_tile(u32x4 (&r)[8], srd_t s, unsigned vo, unsigned so) {
#pragma unroll
  for (int j = 0; j < 8; ++j) r[j] = __builtin_amdgcn_raw_buffer_load_b128(s, vo, so + j * 4 * RS, 0);
}
DI void pv_mfma(f32x16 (&acc)[4], const u32x2 (&f)[4][2][2], u32x4 pb0, u32x4 pb1) {
#pragma unroll
  for (int c = 0; c < 4; ++c) acc[c] = MFMA(cat(f[c][0][0], f[c][0][1]), asbf(pb0), acc[c]);
#pragma unroll
  for (int c = 0; c < 4; ++c) acc[c] = MFMA(cat(f[c][1][0], f[c][1][1]), asbf(pb1), acc[c]);
}
DI void mk_ka(unsigned (&ka)[8], unsigned base, unsigned l, unsigned sx) {
  const unsigned r = l & 31, h = l >> 5, fr = ((r & 3) << 2) | ((r >> 2) & 3);
#pragma unroll
  for (unsigned s = 0; s < 8; ++s) ka[s] = base + 256 * r + 16 * ((2 * (s ^ sx) + h) ^ fr);
}
DI void mk_va(unsigned (&va)[4][2], unsigned base, unsigned l, unsigned w) {
  const unsigned h = l >> 5, blk = (l >> 4) & 1, q = (l & 15) >> 2, p = l & 3;
#pragma unroll
  for (unsigned c = 0; c < 4; ++c)
#pragma unroll
    for (unsigned t = 0; t < 2; ++t) va[c][t] = base + 256 * (8 * t + 4 * h + q) + 16 * (((c ^ w ^ q) << 2) | ((2 * blk + (p >> 1)) ^ (2 * t + h))) + 8 * (p & 1);
}
DI unsigned mk_vo(unsigned l, unsigned j) { return (l >> 4) * 256 + 16 * ((l & 15) ^ ((l >> 4) << 2) ^ j); }
DI void st_tile(char* p, const f32x16& a) {
#pragma unroll
  for (int i = 0; i < 4; ++i) *(f32x4*)(p + i * 1024) = f32x4{a[4 * i], a[4 * i + 1], a[4 * i + 2], a[4 * i + 3]};
}
DI void add_tile(f32x16& a, const char* p, float f) {
#pragma unroll
  for (int i = 0; i < 4; ++i) { const f32x4 v = *(const f32x4*)(p + i * 1024); a[4 * i] += f * v[0]; a[4 * i + 1] += f * v[1]; a[4 * i + 2] += f * v[2]; a[4 * i + 3] += f * v[3]; }
}
DI void qk_softmax(const u32x4 (&kf)[8], const u32x4 (&qf)[8], f32x16 (&acc)[4], float& m, float& lsum, u32x4& p0, u32x4& p1) {
  f32x16 s = {};
#pragma unroll
  for (int i = 0; i < 8; ++i) s = MFMA(asbf(kf[i]), asbf(qf[i]), s);
  float mx = fmaxf(s[0], s[1]);
#pragma unroll
  for (int r = 2; r < 16; r += 2) mx = fmaxf(fmaxf(mx, s[r]), s[r + 1]);
  mx = swap32_max(mx);
  if (__any((mx - m) * SCL > THR)) {
    const float mn = fmaxf(m, mx), alpha = __builtin_amdgcn_exp2f((m - mn) * SCL); m = mn; lsum *= alpha;
#pragma unroll
    for (int c = 0; c < 4; ++c) acc[c] *= alpha;
  }
  const float nm = -m * SCL; float ps0 = 0.f, ps1 = 0.f;
#pragma unroll
  for (int r = 0; r < 16; r += 2) { s[r] = __builtin_amdgcn_exp2f(fmaf(s[r], SCL, nm)); s[r + 1] = __builtin_amdgcn_exp2f(fmaf(s[r + 1], SCL, nm)); ps0 += s[r]; ps1 += s[r + 1]; }
  lsum += ps0 + ps1;
  p0 = pack8(s, 0); p1 = pack8(s, 1);
}

DI u64 get_nonce(const Params& p, int b) { u64 e; asm volatile("global_load_dwordx2 %0, %1, %2 sc1" : "=v"(e) : "v"(0u), "s"(p.ws + WS_E + b * 256) : "memory"); return e; }
DI u64 fix_nonce(u64 e) {
  asm volatile("" : "+v"(e) :: "memory");
  u64 n = (((u64)(unsigned)__builtin_amdgcn_readfirstlane((unsigned)(e >> 32)) << 32) | (unsigned)__builtin_amdgcn_readfirstlane((unsigned)e)) + 0x9E3779B97F4A7C15ull;
  asm volatile("" : "+s"(n)); return n;
}
DI void wait_until(u64 t0, unsigned ticks) { while (__builtin_amdgcn_s_memrealtime() - t0 < ticks) __builtin_amdgcn_s_sleep(8); }
template <int NS> DI void merge(const u32x4 (&v)[NS][2], const f32x2 (&st)[NS], f32x16& o, float& M, float& L) {
  M = st[0][0];
#pragma unroll
  for (int s = 1; s < NS; ++s) M = fmaxf(M, st[s][0]);
  float e[NS]; L = 0.f;
#pragma unroll
  for (int s = 0; s < NS; ++s) { e[s] = st[s][1] * __builtin_amdgcn_exp2f((st[s][0] - M) * SCL); L += e[s]; }
  const float inv = 1.f / L; o = f32x16{};
#pragma unroll
  for (int s = 0; s < NS; ++s) { const float f = e[s] * inv;
#pragma unroll
    for (int j = 0; j < 8; ++j) { o[2 * j] += f * bflo(v[s][j >> 2][j & 3]); o[2 * j + 1] += f * bfhi(v[s][j >> 2][j & 3]); } }
}
template <int N> DI void sum_out(const u32x4* v, srd_t so, unsigned op, unsigned ob) {
  float s[8] = {};
  unsigned c0 = 0x00003f80u, c1 = 0x3f800000u; asm volatile("" : "+s"(c0), "+s"(c1));
#pragma unroll
  for (int i = 0; i < N; ++i)
#pragma unroll
    for (int j = 0; j < 4; ++j) { const unsigned u = v[i][j];
      s[2 * j] = __builtin_amdgcn_fdot2_f32_bf16(__builtin_bit_cast(bf16x2, u), __builtin_bit_cast(bf16x2, c0), s[2 * j], false);
      s[2 * j + 1] = __builtin_amdgcn_fdot2_f32_bf16(__builtin_bit_cast(bf16x2, u), __builtin_bit_cast(bf16x2, c1), s[2 * j + 1], false); }
#pragma unroll
  for (int j = 0; j < 4; ++j) { auto r = __builtin_amdgcn_permlane16_swap(__float_as_uint(s[j]), __float_as_uint(s[j + 4]), false, false); s[j] = __uint_as_float(r[0]) + __uint_as_float(r[1]); }
#pragma unroll
  for (int j = 0; j < 2; ++j) { auto r = __builtin_amdgcn_permlane32_swap(__float_as_uint(s[j]), __float_as_uint(s[j + 2]), false, false); s[j] = __uint_as_float(r[0]) + __uint_as_float(r[1]); }
  __builtin_amdgcn_raw_buffer_store_b32(cvtpk(s[0], s[1]), so, op, ob, 0);
}
DI void final_sum(const Params& p, srd_t sws, int b, int h, int w, unsigned l, u64 nonce) {
  const srd_t so = mksrd(p.out, 524288);
  const unsigned lg = l >> 4, vo = lg * 32768 + (l & 15) * 16 + w * 256, fo = WS_FL + (b * 4 + (h >> 3)) * 512 + (l < 40 ? l : 39) * 8;
  const unsigned vob = lg == 3 ? 0x40000000u : vo;
  const unsigned op = (l & 15) * 2048 + ((l >> 4) & 1) * 16 + (l >> 5) * 4, ob = (b * 32 + 16 * (w & 1)) * 2048 + (16 * h + 4 * (w >> 1)) * 2;
  u32x4 v[10]; unsigned got = 0;
#pragma unroll
  for (int i = 0; i < 10; ++i) v[i] = u32x4{0, 0, 0, 0};
  for (unsigned spins = 0; spins < SPIN_CAP && (got & 1023) != 1023; ++spins) {
    const u64 up = __ballot(__hip_atomic_load((GAS u64*)(p.ws + fo), __ATOMIC_RELAXED, __HIP_MEMORY_SCOPE_AGENT) == nonce);
    asm volatile("" ::: "memory");
    if ((got >> 8) == 3) { got |= 1024; sum_out<2>(v + 8, so, op, ob); }
#pragma unroll
    for (int i = 0; i < 10; ++i)
      if (((up >> (4 * i)) & (i < 8 ? 7 : 15)) == (i < 8 ? 7 : 15) && !(got & (1u << i))) { got |= 1u << i;
        v[i] = __builtin_amdgcn_raw_buffer_load_b128(sws, (i < 8 ? vob : vo) + ((h + (i < 8 ? 4 * i + lg : 16 * (i - 8) + 4 * lg)) & 31) * 1024,
                                                     i < 8 ? WS_PB + (unsigned)(b * 32 + 4 * i) * 32768 : WS_PA + (unsigned)(b * 8 + 4 * (i - 8)) * 32768, SC1); }
  }
  sum_out<8>(v, so, op, ob + 1024);
  if (!(got & 1024)) sum_out<2>(v + 8, so, op, ob);
  if (h == 0 && w == 0 && l == 0) __hip_atomic_store((GAS u64*)(p.ws + WS_E + b * 256), nonce, __ATOMIC_RELAXED, __HIP_MEMORY_SCOPE_AGENT);
}

DI void l2_touch(unsigned lds0, srd_t s, unsigned vo, unsigned so) {
  asm volatile("s_mov_b32 m0, %0\n\ts_nop 4\n\tbuffer_load_dword %1, %2, %3 offen lds" :: "s"(lds0 + JUNK), "v"(vo), "s"(s), "s"(so) : "memory");
}
DI void l2_warm(const Params& p, unsigned lds0, int part, int x, unsigned l) {
  const bool q = part < 64, o = part < 32;
  l2_touch(lds0, mksrd((const void*)(q ? (u64)p.wqb : (u64)p.wob), 4096 * 1024), q ? (l >> 3) * 8192 + (l & 7) * 128 : l * 128,
           __builtin_amdgcn_readfirstlane(q ? part * 65536 + x * 1024 : x * 524288 + (part - 64) * 8192));
  l2_touch(lds0, mksrd((const void*)(o ? (u64)p.wob : (u64)p.woa), 4096 * 1024), l * 128, __builtin_amdgcn_readfirstlane(x * 524288 + (o ? part + 32 : part - 32) * 8192));
}
DI void s_weights(float (&f)[4], char* smem, int w, unsigned l, float m, float lsum) {
  f32x2 st[4]; float M = m;
#pragma unroll
  for (int k = 1; k < 4; ++k) { st[k] = *(const f32x2*)(smem + STATS + (w ^ k) * 256 + (l & 31) * 8); M = fmaxf(M, st[k][0]); }
  f[0] = __builtin_amdgcn_exp2f((m - M) * SCL); float L = f[0] * lsum;
#pragma unroll
  for (int k = 1; k < 4; ++k) { f[k] = __builtin_amdgcn_exp2f((st[k][0] - M) * SCL); L += f[k] * st[k][1]; }
  const float inv = __builtin_amdgcn_rcpf(L);
#pragma unroll
  for (int k = 0; k < 4; ++k) f[k] *= inv;
}
DI void role_s(const Params& p, char* smem, unsigned lds0, int w, unsigned l, int x, int b, int g) {
  const u64 k3 = (u64)p.kb + (size_t)(b * 32 + 4 * x + 3) * 524288, v3 = (u64)p.vb + (size_t)(b * 32 + 4 * x + 3) * 524288;
  const u64 k1 = (u64)p.kb + (size_t)(b * 32 + 4 * x + g) * 524288, v1 = (u64)p.vb + (size_t)(b * 32 + 4 * x + g) * 524288;
  const int len = g == 2 ? 22 : 21, n1 = (len - w + 3) >> 2, n = n1 + 16;
  const unsigned o3 = (21 * g + w) * 8192, o1 = (((w - len) & 3) - 4 * n1) * 8192;
  const unsigned ring = lds0 + SR + w * 16384;
  unsigned vo[4];
#pragma unroll
  for (int j = 0; j < 4; ++j) vo[j] = mk_vo(l, j);
  const bool wfirst = (b & 1) == 0;
  const unsigned ts = (unsigned)__builtin_amdgcn_s_memrealtime();
  if (!wfirst) { dma_kv(ring, vo, k3, o3); dma_kv(ring + 8192, vo, v3, o3); asm volatile("s_barrier" ::: "memory"); }
  asm volatile("s_barrier" ::: "memory");
  if (wfirst) { dma_kv(ring, vo, k3, o3); dma_kv(ring + 8192, vo, v3, o3); l2_warm(p, lds0, (b * 3 + g) * 4 + w, x, l); }
  f32x16 acc[4] = {}; float m = -1e30f, lsum = 0.f;
  unsigned ka[8], va[4][2]; mk_ka(ka, ring, l, 0); mk_va(va, ring, l, w);
  lds_wait<1>(smem, C_Q0, 4);
  u32x4 qf[8];
#pragma unroll
  for (int s = 0; s < 8; ++s) qf[s] = *(const u32x4*)(smem + QF + s * 1024 + l * 16);
  lds_inc(smem, C_Q0R);
  bool earlyV = true, have = false; float f[4] = {};
#pragma unroll 1
  for (int i = 0; i < n; ++i) {
    if (i == n1) {
      lds_wait<1>(smem, C_Q1, 4);
      const unsigned ll = opq(l); lsum = swap32_sum(lsum); const float inv = 1.f / lsum;
#pragma unroll
      for (int c = 0; c < 4; ++c) { acc[c] *= inv;
        *(u32x4*)(smem + CA + w * 8192 + (2 * (c ^ w)) * 1024 + ll * 16) = pack8(acc[c], 0); *(u32x4*)(smem + CA + w * 8192 + (2 * (c ^ w) + 1) * 1024 + ll * 16) = pack8(acc[c], 1);
        acc[c] = f32x16{}; }
      if (ll < 32) *(f32x2*)(smem + STATS2 + w * 256 + ll * 8) = f32x2{m, lsum};
      lds_inc(smem, C_P);
#pragma unroll
      for (int s = 0; s < 8; ++s) qf[s] = *(const u32x4*)(smem + QF + s * 1024 + ll * 16);
      m = -1e30f; lsum = 0.f;
    }
    const bool t0 = i < n1, t1 = i + 1 < n1, more = i + 1 < n;
    const unsigned so = (i * 32768 + (t0 ? o3 : o1)) & 0x7ffff, sn = ((i + 1) * 32768 + (t1 ? o3 : o1)) & 0x7ffff;
    const u64 vc = t0 ? v3 : v1, kn = t1 ? k3 : k1, vn = t1 ? v3 : v1;
    u32x4 kf[8], p0, p1; unsigned hv; u64 now;
    const int k4 = 4 * i + w, due = P_T0 + (k4 < P_K1 ? k4 * P_A : P_K1 * P_A + (k4 - P_K1) * P_B) / 16;
    if (earlyV) waitvm<8>(); else { waitvm<0>(); hold_wait(smem); dma_kv(ring + 8192, vo, vc, so); }
    row_reads_h(kf, ka, hv, lds0 + C_HOLD, now);
    const bool earlyK = more && __builtin_amdgcn_readfirstlane(hv) == 0 && (int)((unsigned)now - ts) >= due;
    if (earlyK) dma_kv(ring, vo, kn, sn);
    SBAR();
    qk_softmax(kf, qf, acc, m, lsum, p0, p1);
    if (!more) {
      lsum = swap32_sum(lsum); *(f32x2*)(smem + STATS + w * 256 + (l & 31) * 8) = f32x2{m, lsum}; lds_inc(smem, C_ST);
      have = __builtin_amdgcn_readfirstlane(__hip_atomic_load((LAS unsigned*)(smem + C_ST), __ATOMIC_RELAXED, __HIP_MEMORY_SCOPE_WORKGROUP)) >= 4;
      asm volatile("" ::: "memory");
      if (have) s_weights(f, smem, w, l, m, lsum);
    }
    SBAR();
    if (earlyK) waitvm<8>(); else { waitvm<0>(); if (more) { hold_wait(smem); dma_kv(ring, vo, kn, sn); } }
    u32x2 vf[4][2][2]; tr_reads_h(vf, va, hv, lds0 + C_HOLD, now);
    earlyV = more && __builtin_amdgcn_readfirstlane(hv) == 0 && (int)((unsigned)now - ts) >= due + 2 * P_A / 16;
    if (earlyV) dma_kv(ring + 8192, vo, vn, sn);
    SBAR();
    pv_mfma(acc, vf, p0, p1);
  }
  l = opq(l);
  if (!have) { lds_wait<0>(smem, C_ST, 4); s_weights(f, smem, w, l, m, lsum); }
  SBAR();
#pragma unroll
  for (int k = 1; k < 4; ++k) st_tile(smem + SR + w * 16384 + (k - 1) * 4096 + l * 16, acc[k]);
  lds_inc(smem, C_SBAR);
  f32x16 o = acc[0] * f[0];
  unsigned a1 = SR + (w ^ 1) * 16384 + l * 16, a2 = SR + (w ^ 2) * 16384 + 4096 + l * 16, a3 = SR + (w ^ 3) * 16384 + 8192 + l * 16;
  asm volatile("" : "+v"(o), "+v"(a1), "+v"(a2), "+v"(a3));
  lds_wait<0>(smem, C_SBAR, 4);
  { f32x4 t[3][4];
#pragma unroll
    for (int i = 0; i < 4; ++i) { t[0][i] = *(const f32x4*)(smem + a1 + i * 1024); t[1][i] = *(const f32x4*)(smem + a2 + i * 1024); t[2][i] = *(const f32x4*)(smem + a3 + i * 1024); }
    SBAR();
#pragma unroll
    for (int k = 0; k < 3; ++k)
#pragma unroll
      for (int i = 0; i < 16; ++i) o[i] += f[k + 1] * t[k][i >> 2][i & 3]; }
  *(u32x4*)(smem + QF + w * 2048 + l * 16) = pack8(o, 0);
  *(u32x4*)(smem + QF + w * 2048 + 1024 + l * 16) = pack8(o, 1);
  lds_inc(smem, C_OB);
}

DI void load_wo(u32x2 (&wf)[4][4][2][2], char* smem, const char* wo, int h, int w, unsigned l, const unsigned (&sa)[4], const unsigned (&va)[4][2]) {
  const srd_t s = mksrd(wo, 4096 * 1024); const unsigned vo = (l >> 4) * 1024 + (l & 15) * 16, so = h * 131072 + w * 256;
  u32x4 r[4][8];
  lds_add(smem, C_HOLD, 256); SBAR();
#pragma unroll
  for (int i = 0; i < 3; ++i) load_tile<1024>(r[i], s, vo, so + i * 32768);
  SBAR();
  stage_tile(r[0], sa); SBAR();
  load_tile<1024>(r[3], s, vo, so + 3 * 32768);
  SBAR(); lds_add(smem, C_HOLD, -256);
  tr_reads<0>(wf[0], va); SBAR();
#pragma unroll
  for (int i = 1; i < 4; ++i) { stage_tile(r[i], sa); tr_reads<0>(wf[i], va); SBAR(); }
}
template <int MODE> DI void q_proj(const Params& p, int part, int x, const char* wq, const char* xb, int h, int rot, bool bar, char* smem, unsigned lds0, int w, unsigned l, unsigned& gen) {
  const srd_t sw = mksrd(wq + h * 256, 512 * 8192 - h * 256), sx = mksrd(xb, 32768);
  u32x4 r[4][8], xf[4][2]; f32x4 acc[8][2] = {};
  { unsigned v1k[4];
#pragma unroll
    for (int j = 0; j < 4; ++j) v1k[j] = mk_vo(l, j) + (l >> 4) * 768 + j * 3072;
    if (MODE == 0) lds_add(smem, C_HOLD, 256);
    dma_tile2<false>(lds0 + STG + w * 8192, v1k, sx, w * 256, w * 256 + 16384); }
#define WLOAD(i) _Pragma("unroll") for (int j = 0; j < 8; ++j) r[i][j] = __builtin_amdgcn_raw_buffer_load_b128(sw, (l >> 4) * 65536 + (l & 15) * 16, (w * 128 + ((i) ^ rot) * 32 + j) * 8192, 0)
  WLOAD(0); WLOAD(1);
  if (MODE == 0) { WLOAD(2); WLOAD(3); SBAR(); lds_add(smem, C_HOLD, -256); waitvm<32>(); } else { SBAR(); waitvm<16>(); }
#pragma unroll
  for (int qb = 0; qb < 2; ++qb) { const unsigned row = (l & 15) + 16 * qb, fr = ((row & 3) << 2) | ((row >> 2) & 3);
#pragma unroll
    for (int i = 0; i < 4; ++i) xf[i][qb] = *(const u32x4*)(smem + STG + w * 8192 + 256 * row + 16 * ((4 * (i ^ rot) + (l >> 4)) ^ fr)); }
#pragma unroll
  for (int i = 0; i < 4; ++i) {
#pragma unroll
    for (int t = 0; t < 8; ++t) {
      u32x4 f;
#pragma unroll
      for (int k = 0; k < 4; ++k) f[k] = __builtin_amdgcn_perm(r[i][2 * k + 1][t >> 1], r[i][2 * k][t >> 1], (t & 1) ? 0x07060302u : 0x05040100u);
      if (MODE == 1 && i == 3 && t == 0) { asm volatile("" : "+v"(f)); if (!bar) l2_warm(p, lds0, part, x, l); }
      acc[t][0] = __builtin_amdgcn_mfma_f32_16x16x32_bf16(asbf(f), asbf(xf[i][0]), acc[t][0], 0, 0, 0);
      acc[t][1] = __builtin_amdgcn_mfma_f32_16x16x32_bf16(asbf(f), asbf(xf[i][1]), acc[t][1], 0, 0, 0);
    }
    SBAR();
    if (MODE == 1 && i == 0) { WLOAD(2); SBAR(); }
    if (MODE == 1 && i == 1) { WLOAD(3); SBAR(); asm volatile("s_barrier" ::: "memory"); }
  }
#undef WLOAD
#pragma unroll
  for (int k = 0; k < 4; ++k) { char* a = smem + (k == w ? STG + w * 8192 : CA + w * 12288 + ((k ^ w) - 1) * 4096) + l * 16;
#pragma unroll
    for (int j = 0; j < 4; ++j) *(f32x4*)(a + j * 1024) = acc[2 * k + (j >> 1)][j & 1]; }
  role_bar(smem, C_XBAR, gen);
  f32x4 t[4][4];
#pragma unroll
  for (int k = 0; k < 4; ++k) { const char* a = smem + (k == w ? STG + w * 8192 : CA + k * 12288 + ((k ^ w) - 1) * 4096) + l * 16;
#pragma unroll
    for (int j = 0; j < 4; ++j) t[k][j] = *(const f32x4*)(a + j * 1024); }
  SBAR();
#pragma unroll
  for (int qb = 0; qb < 2; ++qb) { const f32x4 s0 = (t[0][qb] + t[1][qb]) + (t[2][qb] + t[3][qb]), s1 = (t[0][2 + qb] + t[1][2 + qb]) + (t[2][2 + qb] + t[3][2 + qb]);
#pragma unroll
    for (int e = 0; e < 4; ++e) *(unsigned*)(smem + QF + (2 * (l >> 4) + (e >> 1)) * 1024 + ((e & 1) * 32 + 16 * qb + (l & 15)) * 16 + 4 * w) = cvtpk(s0[e], s1[e]); }
}
DI void role_x(const Params& p, char* smem, unsigned lds0, int w, unsigned l, int x, int b, int g) {
  const int h = 4 * x + g;
  const srd_t sws = mksrd(p.ws, WS_END);
  unsigned gen = 0;
  const u64 t0 = __builtin_amdgcn_s_memrealtime();
  u64 nonce;
  const int part = (b * 3 + g) * 4 + w;
  if (threadIdx.x < 256 + 16) *(LAS unsigned*)(smem + CNT + l * 4) = 0;
  const bool wfirst = (b & 1) == 0;
  const u64 e = get_nonce(p, b);
  if (!wfirst) wg_bar(); else asm volatile("s_waitcnt lgkmcnt(0)" ::: "memory");
  __builtin_amdgcn_s_setprio(1);
  q_proj<1>(p, part, x, p.wqb, p.xb + b * 32768, 4 * x + 3, b >> 1, wfirst, smem, lds0, w, l, gen);
  lds_inc(smem, C_Q0);
  nonce = fix_nonce(e);
  wait_until(t0, T_Q0 + (b * 3 + g) * T_QB);
  lds_wait<1>(smem, C_Q0R, 4);
  q_proj<0>(p, part, x, p.wqb, p.xb + b * 32768, h, 0, false, smem, lds0, w, l, gen);
  lds_inc(smem, C_Q1);
  l = opq(l);
  const unsigned grp = (b * 8 + x) * 4;
  float M, L;
  { lds_wait<4>(smem, C_P, 4);
    u32x4 v[4][2]; f32x2 st[4];
#pragma unroll
    for (int s = 0; s < 4; ++s) { st[s] = *(const f32x2*)(smem + STATS2 + s * 256 + (l & 31) * 8);
      v[s][0] = *(const u32x4*)(smem + CA + s * 8192 + w * 2048 + l * 16); v[s][1] = *(const u32x4*)(smem + CA + s * 8192 + w * 2048 + 1024 + l * 16); }
    f32x16 o; merge<4>(v, st, o, M, L);
    *(u32x4*)(smem + CA + 32768 + (2 * w) * 1024 + l * 16) = pack8(o, 0); *(u32x4*)(smem + CA + 32768 + (2 * w + 1) * 1024 + l * 16) = pack8(o, 1);
    if (w == 0) {
      if (l < 32) __builtin_amdgcn_raw_buffer_store_b64(u32x2{__float_as_uint(M), __float_as_uint(L)}, sws, l * 8, WS_PTS + (grp + g) * 256, SC1);
      flag_set(p.ws, WS_FT + grp * 16 + g * 8, nonce, l);
    }
    role_bar(smem, C_XBAR, gen);
  }
  f32x16 y[4] = {}; u32x2 wf[4][4][2][2]; u32x4 of[8];
  unsigned va[4][2], sa[4]; mk_va(va, lds0 + STG + w * 8192, l, w);
#pragma unroll
  for (int j = 0; j < 4; ++j) sa[j] = lds0 + STG + w * 8192 + j * 1024 + mk_vo(l, j);
  wait_until(t0, T_W3 + (b * 3 + g) * T_W3B);
  load_wo(wf, smem, p.wob, 4 * x + 3, w, l, sa, va);
#pragma unroll
  for (int i = 0; i < 8; ++i) of[i] = *(const u32x4*)(smem + CA + 32768 + i * 1024 + l * 16);
#pragma unroll
  for (int c = 0; c < 4; ++c)
#pragma unroll
    for (int i = 0; i < 8; ++i) y[c] = MFMA(cat(wf[i >> 1][c][i & 1][0], wf[i >> 1][c][i & 1][1]), asbf(of[i]), y[c]);
  { flag_wait<4>(p.ws, WS_FT + grp * 16 + (l < 3 ? l : 2) * 8, nonce);
    f32x2 sg[3];
#pragma unroll
    for (int s = 0; s < 3; ++s) sg[s] = __builtin_bit_cast(f32x2, __builtin_amdgcn_raw_buffer_load_b64(sws, (l & 31) * 8, WS_PTS + (grp + s) * 256, SC1));
    const float Mg = fmaxf(fmaxf(sg[0][0], sg[1][0]), sg[2][0]);
    float Lg = 0.f;
#pragma unroll
    for (int s = 0; s < 3; ++s) Lg += sg[s][1] * __builtin_amdgcn_exp2f((sg[s][0] - Mg) * SCL);
    const float wt = L * __builtin_amdgcn_exp2f((M - Mg) * SCL) / Lg;
#pragma unroll
    for (int c = 0; c < 4; ++c) y[c] *= wt;
  }
  wait_until(t0, T_W0 + (b * 3 + g) * T_WB);
  load_wo(wf, smem, p.wob, h, w, l, sa, va);
  l = opq(l);
  lds_wait<1>(smem, C_OB, 4);
#pragma unroll
  for (int i = 0; i < 8; ++i) of[i] = *(const u32x4*)(smem + QF + i * 1024 + l * 16);
  const unsigned so = WS_PB + (unsigned)(b * 32 + h) * 32768;
#pragma unroll
  for (int c = 0; c < 5; ++c) {
    u32x4 pk;
#pragma unroll
    for (int i = 0; i < 8; ++i) {
      if (c < 4) y[c] = MFMA(cat(wf[i >> 1][c][i & 1][0], wf[i >> 1][c][i & 1][1]), asbf(of[i]), y[c]);
      if (c > 0) { pk[i & 3] = cvtpk(y[c - 1][2 * i], y[c - 1][2 * i + 1]);
        if ((i & 3) == 3) st16(pk, sws, l * 16, so + ((8 * w + 2 * ((c - 1) ^ w) + (i >> 2) + h) & 31) * 1024); }
      SBAR();
    }
  }
  flag_set(p.ws, WS_FL + (b * 4 + w) * 512 + h * 8, nonce, l);
  final_sum(p, sws, b, h, w, opq(l), nonce);
}

DI void role_a(const Params& p, char* smem, unsigned lds0, int v, unsigned l, int x, int b) {
  const int w = v & 3, u = v >> 2;
  const srd_t sws = mksrd(p.ws, WS_END);
  const unsigned wsl = A_WS + v * 8192;
  unsigned va[4][2], vo[4], v1k[4];
#pragma unroll
  for (int j = 0; j < 4; ++j) { vo[j] = mk_vo(l, j); v1k[j] = vo[j] + (l >> 4) * 768 + j * 3072; }
  const u64 kvb = (u64)((v & 2) ? p.va : p.ka) + (size_t)(b * 8 + x) * 524288;
  const srd_t skv = mksrd((const void*)kvb, 524288); const unsigned kvm = lds0 + v * 4096, kvo = u * 8192 + (v & 1) * 4096;
  f32x16 acc[4] = {};
  u32x4 qf[8];
  u64 nonce;
  { const srd_t sx = mksrd(p.xa + b * 32768, 32768), sw = mksrd(p.wqa + (4 * x + w) * 256, 512 * 8192 - (4 * x + w) * 256);
    unsigned v8k[4];
#pragma unroll
    for (int j = 0; j < 4; ++j) v8k[j] = vo[j] + (l >> 4) * 7936 + (((j & 1) << 1) | (j >> 1)) * 32768 - j * 1024;
    const u64 e = get_nonce(p, b);
    dma_half<false>(lds0 + 65536 + v * 4096, v1k, sx, (v >> 1) * 256 + (v & 1) * 16384);
    const int rot = b;
#define WQ_TILE(slot, i) dma_tile2<false>(lds0 + (slot), v8k, sw, (u * 256 + ((i) ^ rot) * 32) * 8192, (u * 256 + ((i) ^ rot) * 32 + 16) * 8192)
    WQ_TILE(v * 8192, 0); WQ_TILE(wsl, 1);
    waitvm<16>(); wg_bar();
    u32x4 xf[2][8];
    { unsigned xa[8]; mk_ka(xa, lds0 + 65536 + u * 16384 + (rot >> 2) * 8192, l, 2 * (rot & 3)); row_reads<0>(xf[0], xa);
      mk_ka(xa, lds0 + 65536 + u * 16384 + ((rot >> 2) ^ 1) * 8192, l, 2 * (rot & 3)); row_reads<0>(xf[1], xa); }
    nonce = fix_nonce(e);
    unsigned vb[4][2]; mk_va(va, lds0 + wsl, l, 2 * u); mk_va(vb, lds0 + v * 8192, l, 2 * u);
#pragma unroll
    for (int i = 0; i < 8; ++i) {
      u32x2 vf[4][2][2];
      waitvm<8>();
      if (i & 1) tr_reads<0>(vf, va); else tr_reads<0>(vf, vb);
      if (i < 6) { if (i & 1) WQ_TILE(wsl, i + 2); else WQ_TILE(v * 8192, i + 2); }
      else if (i == 6) { wg_bar(); dma_half<true>(kvm, vo, skv, kvo); dma_half<true>(kvm + 32768, vo, skv, kvo + 16384); }
      SBAR(); pv_mfma(acc, vf, xf[i >> 2][2 * (i & 3)], xf[i >> 2][2 * (i & 3) + 1]); SBAR();
    }
#undef WQ_TILE
    char* a0 = smem + wsl + l * 16; char* a1 = smem + A_WS + (v ^ 4) * 8192 + l * 16;
    st_tile(a0, acc[2]); st_tile(a0 + 4096, acc[3]);
    wg_bar();
    add_tile(acc[0], a1, 1.f); add_tile(acc[1], a1 + 4096, 1.f);
#pragma unroll
    for (int s = 0; s < 4; ++s) { qf[s] = natural(pack8(acc[s >> 1], s & 1)); *(u32x4*)(a1 + s * 1024) = qf[s]; }
    waitvm<4>(); wg_bar();
#pragma unroll
    for (int s = 0; s < 4; ++s) qf[4 + s] = *(const u32x4*)(a0 + s * 1024);
    asm volatile("" : "+v"(qf[4]), "+v"(qf[5]), "+v"(qf[6]), "+v"(qf[7]));
  }
  const srd_t swo = mksrd(p.woa + w * 256, 4096 * 1024 - w * 256);
  float m = -1e30f, lsum = 0.f;
  { unsigned ka[8]; mk_ka(ka, lds0 + u * 16384, l, 4 * u); mk_va(va, lds0 + u * 16384, l, 2 * u);
#pragma unroll
    for (int c = 0; c < 4; ++c) acc[c] = f32x16{};
    int slot = 0;
#pragma unroll 1
    for (int s = 0; s < 32; ++s) {
      if (s < 30) dma_half<true>(kvm + (slot == 0 ? 2 : slot - 1) * 32768, vo, skv, kvo + (s + 2) * 16384);
      else if (s == 30 && u == 1) { dma_tile2<false>(lds0 + wsl - 32768, v1k, swo, x * 524288, x * 524288 + 16384); dma_tile2<false>(lds0 + wsl, v1k, swo, x * 524288 + 32768, x * 524288 + 49152); }
      u32x4 kf[8], p0, p1;
      row_reads<0>(kf, ka);
      u32x2 vf[4][2][2]; tr_issue<8192>(vf, va);
      SBAR();
      qk_softmax(kf, qf, acc, m, lsum, p0, p1);
      SBAR();
      tr_wait(vf);
      SBAR();
      pv_mfma(acc, vf, p0, p1);
      const unsigned d = slot == 2 ? -65536 : 32768; slot = slot == 2 ? 0 : slot + 1;
#pragma unroll
      for (int i = 0; i < 8; ++i) { ka[i] += d; va[i >> 1][i & 1] += d; }
      if (s < 30) waitvm<4>(); else if (u == 1) waitvm<16>(); else waitvm<0>();
      wg_bar();
    }
  }
  { l = opq(l);
    lsum = swap32_sum(lsum);
    char* of = smem + A_OF + (w * 8 + 4 * u) * 1024;
    if (l < 32) *(f32x2*)(smem + A_OF + (w * 8 + 4 * (u ^ 1)) * 1024 + l * 8) = f32x2{m, lsum};
    st_tile(smem + v * 8192 + l * 16, acc[2]); st_tile(smem + v * 8192 + 4096 + l * 16, acc[3]);
    wg_bar();
    const f32x2 st = *(const f32x2*)(of + (l & 31) * 8);
    const float M = fmaxf(m, st[0]), f0 = __builtin_amdgcn_exp2f((m - M) * SCL), f1 = __builtin_amdgcn_exp2f((st[0] - M) * SCL), inv = 1.f / (f0 * lsum + f1 * st[1]);
    acc[0] *= f0 * inv; acc[1] *= f0 * inv;
    add_tile(acc[0], smem + (v ^ 4) * 8192 + l * 16, f1 * inv); add_tile(acc[1], smem + (v ^ 4) * 8192 + 4096 + l * 16, f1 * inv);
#pragma unroll
    for (int s = 0; s < 4; ++s) *(u32x4*)(of + s * 1024 + l * 16) = pack8(acc[s >> 1], s & 1);
    waitvm<8>(); wg_bar();
  }
  if (u == 1) {
#pragma unroll 1
    for (int i = 0; i < 16; ++i) {
      if (i < 14) { const unsigned so = x * 524288 + (i + 2) * 32768;
        dma_tile2<false>(lds0 + w * 8192 + (i & 1) * 32768 + ((i & 2) ? A_WS : 0), v1k, swo, so, so + 16384); waitvm<8>(); }
      else waitvm<0>();
      wg_bar();
    }
  } else {
    unsigned vb[4][2]; mk_va(va, lds0 + wsl, l, 0); mk_va(vb, lds0 + w * 8192, l, 0);
#pragma unroll
    for (int c = 0; c < 4; ++c) acc[c] = f32x16{};
#pragma unroll 1
    for (int i = 0; i < 16; i += 4) {
      const char* of = smem + A_OF + i * 2048 + l * 16;
#pragma unroll
      for (int k = 0; k < 4; ++k) {
        u32x2 vf[4][2][2];
        const u32x4 b0 = *(const u32x4*)(of + k * 2048), b1 = *(const u32x4*)(of + k * 2048 + 1024);
        if (k == 0) tr_reads<0>(vf, va); else if (k == 1) tr_reads<32768>(vf, va); else if (k == 2) tr_reads<0>(vf, vb); else tr_reads<32768>(vf, vb);
        asm volatile("s_barrier" ::: "memory");
        SBAR(); pv_mfma(acc, vf, b0, b1); SBAR();
      }
    }
  }
  if (u == 0) {
    const unsigned so = WS_PA + (unsigned)(b * 8 + x) * 32768;
#pragma unroll
    for (int c = 0; c < 4; ++c) {
      st16(pack8(acc[c], 0), sws, l * 16, so + ((8 * w + 2 * c + 4 * x) & 31) * 1024);
      st16(pack8(acc[c], 1), sws, l * 16, so + ((8 * w + 2 * c + 1 + 4 * x) & 31) * 1024);
    }
    flag_set(p.ws, WS_FL + (b * 4 + w) * 512 + 256 + x * 8, nonce, l);
  } else {
    final_sum(p, sws, b, 4 * x + 3, w, l, nonce);
  }
}

__global__ __launch_bounds__(512, 2) void fused_attn_kernel(Params p) {
  extern __shared__ __attribute__((aligned(16))) char smem[];
  const unsigned lds0 = (unsigned)(size_t)(LAS char*)smem, l = threadIdx.x & 63;
  const int wv = __builtin_amdgcn_readfirstlane(threadIdx.x >> 6), wg = blockIdx.x, x = wg & 7, g = (wg >> 3) & 3, b = wg >> 5;
  if (g == 3) role_a(p, smem, lds0, wv, l, x, b);
  else if (wv < 4) role_s(p, smem, lds0, wv, l, x, b, g);
  else role_x(p, smem, lds0, wv - 4, l, x, b, g);
}

extern "C" void kernel_launch(void* const* d_in, const int* in_sizes, int n_in, void* d_out, int out_size, void* d_ws, size_t ws_size, hipStream_t stream) {
  Params p;
  p.xa = (const char*)d_in[0]; p.ka = (const char*)d_in[1]; p.va = (const char*)d_in[2]; p.wqa = (const char*)d_in[3]; p.woa = (const char*)d_in[4];
  p.xb = (const char*)d_in[5]; p.kb = (const char*)d_in[6]; p.vb = (const char*)d_in[7]; p.wqb = (const char*)d_in[8]; p.wob = (const char*)d_in[9];
  p.out = (char*)d_out; p.ws = (char*)d_ws;
  (void)hipFuncSetAttribute((const void*)fused_attn_kernel, hipFuncAttributeMaxDynamicSharedMemorySize, LDS_BYTES);
  fused_attn_kernel<<<256, 512, LDS_BYTES, stream>>>(p);
}
```
